```python
import torch

def _softmax(x, axis=-1):
    return torch.softmax(x, dim=axis)

def _gelu(x):
    return torch.nn.functional.gelu(x, approximate='tanh')

def _silu(x):
    return torch.nn.functional.silu(x)

def _sigmoid(x):
    return torch.sigmoid(x)

def _layernorm(x, axis=-1, eps=1e-05):
    m = x.mean(dim=axis, keepdim=True)
    v = x.var(dim=axis, keepdim=True, unbiased=False)
    return (x - m) / torch.sqrt(v + eps)

def _scatter_add(tgt, idx, src, axis=0):
    return tgt.clone().index_add_(axis, idx, src)

def _segment_sum(src, seg, n):
    out = torch.zeros((n,) + src.shape[1:], dtype=src.dtype)
    return out.index_add_(0, seg, src)

def _pad(x, pw):
    flat = tuple((v for pair in reversed(pw) for v in pair))
    return torch.nn.functional.pad(x, flat)
kernel_name = 'adaLN_cond__softmax_full_mha__mixer_gmlp__prompt'

def setup_inputs():

    _w = lambda a: (lambda t: t.to(torch.bfloat16) if t.is_floating_point() else t)(torch.as_tensor(a))

    def _A_setup():
        B, S, D = (1, 2048, 640)
        return {'x': _w((lambda *s: torch.randn(*s, dtype=torch.bfloat16) * 0.05)(B, S, D)), 'wqkv': _w((lambda *s: torch.randn(*s, dtype=torch.bfloat16) * 0.05)(D, 3 * D)), 'wo': _w((lambda *s: torch.randn(*s, dtype=torch.bfloat16) * 0.05)(D, D))}
    _A_in = _A_setup()

    def _B_setup():
        B, S, D, F = (1, 2048, 640, 1728)
        Fh = F // 2
        return {'x': _w((lambda *s: torch.randn(*s, dtype=torch.bfloat16) * 0.05)(B, S, D)), 'w_in': _w((lambda *s: torch.randn(*s, dtype=torch.bfloat16) * 0.4)(D, 2 * Fh)), 'w_sp': _w(0.01 * (lambda *s: torch.randn(*s, dtype=torch.bfloat16) * 0.4)(S, S)), 'b_sp': _w(1.0 + 0.01 * (lambda *s: torch.randn(*s, dtype=torch.bfloat16) * 0.05)(S)), 'w_out': _w((lambda *s: torch.randn(*s, dtype=torch.bfloat16) * 0.05)(Fh, D))}
    _B_in = _B_setup()
    return {'A_x': _A_in['x'], 'A_wqkv': _A_in['wqkv'], 'A_wo': _A_in['wo'], 'B_x': _B_in['x'], 'B_w_in': _B_in['w_in'], 'B_w_sp': _B_in['w_sp'], 'B_b_sp': _B_in['b_sp'], 'B_w_out': _B_in['w_out'], 'c': _w((lambda *s: torch.randn(*s, dtype=torch.bfloat16) * 0.1)(1, 640) * 8.0), 'w_mod': _w((lambda *s: torch.randn(*s, dtype=torch.bfloat16) * 0.1)(640, 6 * 640) * 640 ** (-0.5) * 8.0)}

def reference(A_x, A_wqkv, A_wo, B_x, B_w_in, B_w_sp, B_b_sp, B_w_out, c, w_mod):

    def _A_ref(x, wqkv, wo):
        B, S, D = x.shape
        H = 32
        Dh = D // H
        qkv = (lambda x, *d: x.permute(*d))((x @ wqkv).reshape(B, S, 3, H, Dh), 2, 0, 3, 1, 4)
        q, k, v = (qkv[0], qkv[1], qkv[2])
        scale = Dh ** (-0.5)
        scores = torch.einsum('bhqd,bhkd->bhqk', q, k) * scale
        p = _softmax(scores, axis=-1)
        o = torch.einsum('bhqk,bhkd->bhqd', p, v)
        return (lambda x, *d: x.permute(*d))(o, 0, 2, 1, 3).reshape(B, S, D) @ wo

    def _B_ref(x, w_in, w_sp, b_sp, w_out):
        B, S, D = x.shape
        F = w_in.shape[-1]
        Fh = F // 2
        z = _gelu(torch.einsum('bsd,df->bsf', x, w_in))
        z1 = z[..., :Fh].clone()
        z2 = _layernorm(z[..., Fh:], axis=-1)
        g = torch.einsum('ts,bsf->btf', w_sp, z2) + b_sp[None, :, None]
        return torch.einsum('bsf,fd->bsd', z1 * g, w_out)
    x = A_x
    mod = (_silu(c) @ w_mod).reshape(c.shape[0], 6, 1, -1)
    sa, ca, ga, sb, cb, gb = (mod[:, 0].clone(), mod[:, 1].clone(), mod[:, 2].clone(), mod[:, 3].clone(), mod[:, 4].clone(), mod[:, 5].clone())
    h = _layernorm(x, axis=-1) * (1 + ca) + sa
    x = x + ga * _A_ref(h, A_wqkv, A_wo)
    h = _layernorm(x, axis=-1) * (1 + cb) + sb
    x = x + gb * _B_ref(h, B_w_in, B_w_sp, B_b_sp, B_w_out)
    return x
```

```cpp
#include <hip/hip_runtime.h>
#include <hip/hip_bf16.h>
#include <stdint.h>

typedef unsigned short u16;
typedef __attribute__((ext_vector_type(8))) __bf16 bf16x8;
typedef __attribute__((ext_vector_type(2))) __bf16 bf16x2;
typedef __attribute__((ext_vector_type(4))) short s16x4;
typedef __attribute__((ext_vector_type(2))) float f32x2;
typedef __attribute__((ext_vector_type(4))) float f32x4;
typedef __attribute__((ext_vector_type(16))) float f32x16;
typedef __attribute__((ext_vector_type(4))) unsigned u32x4;
typedef __attribute__((ext_vector_type(2))) unsigned u32x2;
#define LAS __attribute__((address_space(3)))
#define GAS __attribute__((address_space(1)))
#define DEVI __device__ __forceinline__

constexpr int S = 2048, D = 640, NH = 32, DH = 20, FF = 1728, FH = 864;
constexpr int UP = 896;

constexpr size_t WS_MOD  = 0;
constexpr size_t WS_STAT = 8192;
constexpr size_t WS_WQKV = WS_STAT + 32 * 8 * 64 * 8;
constexpr size_t WS_WO   = WS_WQKV + (size_t)8 * 15 * 20 * 1024;
constexpr size_t WS_WIN  = WS_WO + (size_t)40 * 20 * 1024;
constexpr size_t WS_WOUT = WS_WIN + (size_t)8 * 14 * 20 * 1024;
constexpr size_t WS_QA   = WS_WOUT + (size_t)40 * 28 * 1024;
constexpr size_t WS_QB   = WS_QA + (size_t)NH * S * 32;
constexpr size_t WS_KA   = WS_QB + (size_t)NH * S * 8;
constexpr size_t WS_KB   = WS_KA + (size_t)NH * S * 32;
constexpr size_t WS_VT   = WS_KB + (size_t)NH * S * 8;
constexpr size_t WS_O    = WS_VT + (size_t)D * S * 2;
constexpr size_t WS_X1   = WS_O + (size_t)S * D * 2;
constexpr size_t WS_ZT   = WS_X1 + (size_t)S * D * 2;
constexpr size_t WS_Z1   = WS_ZT + (size_t)8 * 7 * 64 * 1024;
constexpr size_t WS_U    = WS_Z1 + (size_t)32 * 8 * 7 * 4 * 512;
constexpr size_t WS_END  = WS_U + (size_t)S * UP * 2;

DEVI float bf_lo(unsigned w) { return __uint_as_float(w << 16); }
DEVI float bf_hi(unsigned w) { return __uint_as_float(w & 0xffff0000u); }
DEVI float bf2f(u16 h) { return __uint_as_float((unsigned)h << 16); }
DEVI unsigned pk2(float a, float b) { bf16x2 v = {(__bf16)a, (__bf16)b}; return __builtin_bit_cast(unsigned, v); }
DEVI float rbf(float a) { return bf_lo(pk2(a, a)); }
DEVI f32x4 mfma16(bf16x8 a, bf16x8 b, f32x4 c) { return __builtin_amdgcn_mfma_f32_16x16x32_bf16(a, b, c, 0, 0, 0); }
DEVI f32x16 mfma32(bf16x8 a, bf16x8 b, f32x16 c) { return __builtin_amdgcn_mfma_f32_32x32x16_bf16(a, b, c, 0, 0, 0); }
DEVI f32x16 mfma32k8(s16x4 a, s16x4 b, f32x16 c) { return __builtin_amdgcn_mfma_f32_32x32x8bf16_1k(a, b, c, 0, 0, 0); }
DEVI bf16x8 ldg8(const void* p) { return *(const GAS bf16x8*)p; }
DEVI bf16x8 lds8(unsigned a) { return *(const LAS bf16x8*)a; }
DEVI void glds16(const void* g, unsigned lds_base) {
  unsigned sv;
  asm volatile("s_mov_b32 %0, m0\n\ts_mov_b32 m0, %2\n\ts_nop 0\n\tglobal_load_lds_dwordx4 %1, off\n\ts_mov_b32 m0, %0"
               : "=&s"(sv) : "v"(g), "s"(lds_base) : "memory");
}
#define WAITV(n) asm volatile("s_waitcnt vmcnt(%0)" ::"i"(n) : "memory")
#define BAR() asm volatile("s_waitcnt lgkmcnt(0)\n\ts_barrier" ::: "memory")
DEVI int ufl(int x) { return __builtin_amdgcn_readfirstlane(x); }
template <int CTRL> DEVI float dpp_add(float v) {
  return v + __builtin_bit_cast(float, __builtin_amdgcn_update_dpp(0, __builtin_bit_cast(int, v), CTRL, 0xf, 0xf, true));
}
DEVI float row16_sum(float v) {
  v = dpp_add<0x128>(v);
  v = dpp_add<0x124>(v);
  v = dpp_add<0x122>(v);
  v = dpp_add<0x121>(v);
  return v;
}
DEVI float gelu_tanh(float x) {
  const float c1 = -2.0f * 0.7978845608028654f * 1.4426950408889634f, c3 = c1 * 0.044715f;
  float e = __builtin_amdgcn_exp2f(x * __builtin_fmaf(x * x, c3, c1));
  return x * __builtin_amdgcn_rcpf(1.0f + e);
}

DEVI void tile_frag(const u16* __restrict__ W, int ldw, int k0, int n, bool ok_n, int kmap, u16* dst, int lane) {
  unsigned r[4];
#pragma unroll
  for (int jj = 0; jj < 4; ++jj) {
    unsigned lo = 0, hi = 0;
    int ka = k0 + 8 * (lane >> 4) + 2 * jj;
    bool ok = ok_n;
    int kr = ka;
    if (kmap) { int g = ka / 112, o = ka - g * 112; ok = ok && o < 108; kr = g * 108 + o; }
    if (ok) { lo = W[(size_t)kr * ldw + n]; hi = W[(size_t)(kr + 1) * ldw + n]; }
    r[jj] = lo | (hi << 16);
  }
  *(u32x4*)(dst + lane * 8) = u32x4{r[0], r[1], r[2], r[3]};
}

__global__ __launch_bounds__(256) void k_prep(const u16* __restrict__ c, const u16* __restrict__ w_mod, const u16* __restrict__ wqkv,
                                              const u16* __restrict__ wo, const u16* __restrict__ w_in, const u16* __restrict__ w_out,
                                              char* __restrict__ ws) {
  __shared__ float red[4][16];
  const int tid = threadIdx.x, lane = tid & 63, wv = tid >> 6, b = blockIdx.x;
  if (b < 240) {
    const int cb = (b & 7) * 30 + (b >> 3);
    const int half = tid & 1, slot = tid >> 1;
    u32x4 v[5]; float sc[5];
#pragma unroll
    for (int i = 0; i < 5; ++i) {
      int k = slot + 128 * i;
      v[i] = __builtin_nontemporal_load((const u32x4*)(w_mod + (size_t)k * (6 * D) + cb * 16 + half * 8));
      float x = bf2f(c[k]);
      sc[i] = rbf(x * __builtin_amdgcn_rcpf(1.0f + __builtin_amdgcn_exp2f(-1.4426950408889634f * x)));
    }
    float a[8];
#pragma unroll
    for (int j = 0; j < 8; ++j) a[j] = 0.f;
#pragma unroll
    for (int i = 0; i < 5; ++i)
#pragma unroll
      for (int j = 0; j < 4; ++j) { a[2 * j] += sc[i] * bf_lo(v[i][j]); a[2 * j + 1] += sc[i] * bf_hi(v[i][j]); }
#pragma unroll
    for (int j = 0; j < 8; ++j) {
#pragma unroll
      for (int o = 2; o < 64; o <<= 1) a[j] += __shfl_xor(a[j], o);
    }
    if (lane < 2) {
#pragma unroll
      for (int j = 0; j < 8; ++j) red[wv][lane * 8 + j] = a[j];
    }
    __syncthreads();
    if (tid < 16) {
      float s = (red[0][tid] + red[1][tid]) + (red[2][tid] + red[3][tid]);
      int col = cb * 16 + tid, seg = col / D;
      s = rbf(s);
      if (seg == 1 || seg == 4) s = 1.0f + s;
      ((u16*)(ws + WS_MOD))[col] = (u16)(pk2(s, s) & 0xffffu);
    }
    return;
  }
  if (b < 256) {
    u32x4* p = (u32x4*)(ws + WS_STAT) + (b - 240) * 512;
    p[tid] = u32x4{~0u, ~0u, ~0u, ~0u}; p[tid + 256] = u32x4{~0u, ~0u, ~0u, ~0u};
    return;
  }
  int f = (b - 256) * 4 + wv;
  const int nl = lane & 15;
  if (f < 2400) {
    int t = f / 20, ks = f - t * 20, j = t / 15, tt = t - j * 15, seg = tt / 5, tl = tt - seg * 5;
    tile_frag(wqkv, 3 * D, ks * 32, seg * D + 80 * j + 16 * tl + nl, true, 0, (u16*)(ws + WS_WQKV) + (size_t)f * 512, lane);
    return;
  }
  f -= 2400;
  if (f < 800) {
    int t = f / 20, ks = f - t * 20;
    tile_frag(wo, D, ks * 32, 16 * t + nl, true, 0, (u16*)(ws + WS_WO) + (size_t)f * 512, lane);
    return;
  }
  f -= 800;
  if (f < 2240) {
    int t = f / 20, ks = f - t * 20, j = t / 14, tt = t - j * 14, seg = tt / 7, tl = tt - seg * 7;
    int o = 16 * tl + nl;
    tile_frag(w_in, FF, ks * 32, (seg == 0 ? FH : 0) + 108 * j + o, o < 108, 0, (u16*)(ws + WS_WIN) + (size_t)f * 512, lane);
    return;
  }
  f -= 2240;
  if (f < 1120) {
    int t = f / 28, ks = f - t * 28;
    tile_frag(w_out, D, ks * 32, 16 * t + nl, true, 1, (u16*)(ws + WS_WOUT) + (size_t)f * 512, lane);
  }
}

struct LnRegs { u32x4 x[2][5]; u32x4 g[5], s[5]; };
DEVI void ln_load(LnRegs& R, const u16* __restrict__ xp, const u16* __restrict__ gp, const u16* __restrict__ sp, int wv, int lane) {
  const int rr = lane >> 4, c = lane & 15;
#pragma unroll
  for (int p = 0; p < 2; ++p)
#pragma unroll
    for (int i = 0; i < 5; ++i) R.x[p][i] = *(const u32x4*)(xp + (size_t)(wv * 8 + p * 4 + rr) * D + (c + 16 * i) * 8);
#pragma unroll
  for (int i = 0; i < 5; ++i) { R.g[i] = *(const u32x4*)(gp + (c + 16 * i) * 8); R.s[i] = *(const u32x4*)(sp + (c + 16 * i) * 8); }
}
DEVI void ln_store(const LnRegs& R, unsigned lds0, int wv, int lane) {
  const int rr = lane >> 4, c = lane & 15;
#pragma unroll
  for (int p = 0; p < 2; ++p) {
    float s1 = 0.f;
#pragma unroll
    for (int i = 0; i < 5; ++i)
#pragma unroll
      for (int j = 0; j < 4; ++j) s1 += bf_lo(R.x[p][i][j]) + bf_hi(R.x[p][i][j]);
    const float mu = row16_sum(s1) * (1.0f / D);
    float s2 = 0.f;
#pragma unroll
    for (int i = 0; i < 5; ++i)
#pragma unroll
      for (int j = 0; j < 4; ++j) { float a = bf_lo(R.x[p][i][j]) - mu, b = bf_hi(R.x[p][i][j]) - mu; s2 = __builtin_fmaf(a, a, s2); s2 = __builtin_fmaf(b, b, s2); }
    const float rs = __builtin_amdgcn_rsqf(row16_sum(s2) * (1.0f / D) + 1e-5f);
    const float nb = -mu * rs;
    const int row = wv * 8 + p * 4 + rr;
#pragma unroll
    for (int i = 0; i < 5; ++i) {
      u32x4 o;
#pragma unroll
      for (int j = 0; j < 4; ++j) {
        float a = __builtin_fmaf(bf_lo(R.x[p][i][j]), rs, nb), b = __builtin_fmaf(bf_hi(R.x[p][i][j]), rs, nb);
        a = __builtin_fmaf(a, bf_lo(R.g[i][j]), bf_lo(R.s[i][j])); b = __builtin_fmaf(b, bf_hi(R.g[i][j]), bf_hi(R.s[i][j]));
        o[j] = pk2(a, b);
      }
      const int cg = c + 16 * i;
      *(LAS u32x4*)(lds0 + (cg >> 3) * 8192 + row * 128 + (((cg & 7) ^ (row & 7)) << 4)) = o;
    }
  }
}
DEVI unsigned afrag_base(unsigned lds0, int lane, int odd) {
  const int r = lane & 15, g = lane >> 4;
  return lds0 + r * 128 + (((4 * odd + g) ^ (r & 7)) << 4);
}

template <bool VORI>
DEVI void qkv_loop(f32x4 (&acc)[2][4], const char* wp, unsigned a0, unsigned a1, bool two) {
#pragma unroll 4
  for (int ks = 0; ks < 20; ++ks) {
    bf16x8 b0 = ldg8(wp + ks * 1024);
    bf16x8 b1 = ldg8(wp + (two ? 20 * 1024 : 0) + ks * 1024);
    const unsigned ab = ((ks & 1) ? a1 : a0) + (ks >> 1) * 8192;
#pragma unroll
    for (int mt = 0; mt < 4; ++mt) {
      bf16x8 a = lds8(ab + mt * 2048);
      if (VORI) { acc[0][mt] = mfma16(a, b0, acc[0][mt]); acc[1][mt] = mfma16(a, b1, acc[1][mt]); }
      else      { acc[0][mt] = mfma16(b0, a, acc[0][mt]); acc[1][mt] = mfma16(b1, a, acc[1][mt]); }
    }
  }
}
__global__ __launch_bounds__(512, 2) void k_qkv(const u16* __restrict__ x, char* __restrict__ ws) {
  extern __shared__ __attribute__((aligned(16))) char smem[];
  const int tid = threadIdx.x, lane = tid & 63, wv = ufl(tid >> 6), j = blockIdx.x & 7, pi = blockIdx.x >> 3;
  const unsigned lds0 = (unsigned)(uintptr_t)smem;
  const u16* mod = (const u16*)(ws + WS_MOD);
  LnRegs R;
  ln_load(R, x + (size_t)pi * 64 * D, mod + D, mod, wv, lane);
  ln_store(R, lds0, wv, lane);
  BAR();
  f32x4 acc[2][4] = {};
  const char* wp = ws + WS_WQKV + (size_t)(j * 15 + 2 * wv) * 20 * 1024 + lane * 16;
  const unsigned a0 = afrag_base(lds0, lane, 0), a1 = afrag_base(lds0, lane, 1);
  const int r = lane & 15, g = lane >> 4;
  if (wv < 5) {
    qkv_loop<false>(acc, wp, a0, a1, true);
#pragma unroll
    for (int t = 0; t < 2; ++t) {
      const int tile = 2 * wv + t, isk = tile >= 5, tl = tile - 5 * isk;
      const int nl = 16 * tl + 4 * g, hh = nl / DH, d = nl - hh * DH, head = 4 * j + hh;
      const float sc = isk ? 1.0f : 0.22360679774997896f * 1.4426950408889634f;
      char* pa = ws + (isk ? WS_KA : WS_QA) + ((size_t)(head * 2 + (d >> 3)) * S) * 16 + (d & 7) * 2;
      char* pb = ws + (isk ? WS_KB : WS_QB) + (size_t)head * S * 8;
#pragma unroll
      for (int mt = 0; mt < 4; ++mt) {
        const int s = pi * 64 + mt * 16 + r;
        u32x2 o = {pk2(acc[t][mt][0] * sc, acc[t][mt][1] * sc), pk2(acc[t][mt][2] * sc, acc[t][mt][3] * sc)};
        char* p = d < 16 ? pa + (size_t)s * 16 : pb + (size_t)s * 8;
        *(u32x2*)p = o;
      }
    }
  } else {
    qkv_loop<true>(acc, wp, a0, a1, wv < 7);
#pragma unroll
    for (int t = 0; t < 2; ++t) {
      if (t == 1 && wv == 7) break;
      const int tl = 2 * wv + t - 10;
      char* p = ws + WS_VT + ((size_t)(80 * j + 16 * tl + r) * S + pi * 64 + 4 * g) * 2;
#pragma unroll
      for (int mt = 0; mt < 4; ++mt)
        *(u32x2*)(p + mt * 32) = u32x2{pk2(acc[t][mt][0], acc[t][mt][1]), pk2(acc[t][mt][2], acc[t][mt][3])};
    }
  }
}

constexpr int AT_C = 512;
constexpr int AT_KB = 2 * AT_C * 16, AT_V = AT_KB + AT_C * 8, AT_RV = AT_C * 2 + 16, AT_SLOT = AT_V + 21 * AT_RV;
constexpr int AT_LDS = 3 * AT_SLOT;
DEVI float max16(const f32x16& p) {
  float a = __builtin_fmaxf(__builtin_fmaxf(p[0], p[1]), p[2]), b = __builtin_fmaxf(__builtin_fmaxf(p[3], p[4]), p[5]);
  a = __builtin_fmaxf(__builtin_fmaxf(a, p[6]), p[7]); b = __builtin_fmaxf(__builtin_fmaxf(b, p[8]), p[9]);
  a = __builtin_fmaxf(__builtin_fmaxf(a, p[10]), p[11]); b = __builtin_fmaxf(__builtin_fmaxf(b, p[12]), p[13]);
  a = __builtin_fmaxf(__builtin_fmaxf(a, p[14]), p[15]);
  float m = __builtin_fmaxf(a, b);
  auto rr = __builtin_amdgcn_permlane32_swap(__float_as_uint(m), __float_as_uint(m), false, false);
  return __builtin_fmaxf(__uint_as_float(rr[0]), __uint_as_float(rr[1]));
}
DEVI bf16x8 pack8(const f32x16& p, int o) {
  u32x4 w = {pk2(p[o], p[o + 1]), pk2(p[o + 2], p[o + 3]), pk2(p[o + 4], p[o + 5]), pk2(p[o + 6], p[o + 7])};
  return __builtin_bit_cast(bf16x8, w);
}
DEVI void exp16(f32x16& p) {
#pragma unroll
  for (int r = 0; r < 16; ++r) p[r] = __builtin_amdgcn_exp2f(p[r]);
}
__global__ __launch_bounds__(512, 2) void k_attn(char* __restrict__ ws) {
  extern __shared__ __attribute__((aligned(16))) char smem[];
  const int tid = threadIdx.x, lane = tid & 63, wv = ufl(tid >> 6), r32 = lane & 31, hi = lane >> 5;
  const int head = (blockIdx.x & 7) * 4 + ((blockIdx.x >> 3) & 3), qb = blockIdx.x >> 5;
  const unsigned lds0 = (unsigned)(uintptr_t)smem;
  const char* KA = ws + WS_KA + (size_t)head * S * 32;
  const char* KB = ws + WS_KB + (size_t)head * S * 8;
  const char* VT = ws + WS_VT + (size_t)head * DH * S * 2;
  auto stage = [&](int ch) {
    const unsigned sl = lds0 + (ch % 3) * AT_SLOT;
    glds16(KA + (size_t)(ch * AT_C + wv * 64 + lane) * 16, sl + wv * 1024);
    glds16(KA + (size_t)(S + ch * AT_C + wv * 64 + lane) * 16, sl + AT_C * 16 + wv * 1024);
#pragma unroll
    for (int i = 0; i < 3; ++i) {
      const int id = 3 * wv + i;
      const char* src = id < 4 ? KB + (size_t)ch * AT_C * 8 + id * 1024 : VT + ((size_t)(id - 4) * S + ch * AT_C) * 2;
      const unsigned dst = id < 4 ? sl + AT_KB + id * 1024 : sl + AT_V + (id - 4) * AT_RV;
      glds16(src + lane * 16, dst);
    }
  };
  stage(0);
  const int q = qb * 256 + wv * 32 + r32;
  const bf16x8 qa = ldg8(ws + WS_QA + ((size_t)(head * 2 + hi) * S + q) * 16);
  u32x2 qbw = *(const u32x2*)(ws + WS_QB + ((size_t)head * S + q) * 8);
  stage(1); stage(2);
  if (hi) qbw = u32x2{0u, 0u};
  const s16x4 qb4 = __builtin_bit_cast(s16x4, qbw);
  if (tid < 192) *(LAS u32x4*)(lds0 + (tid >> 6) * AT_SLOT + AT_V + 20 * AT_RV + lane * 16) = u32x4{0x3f803f80u, 0x3f803f80u, 0x3f803f80u, 0x3f803f80u};
  const int pk = (r32 & 0x13) | ((r32 & 4) << 1) | ((r32 & 8) >> 1);
  const int vrow = r32 <= 20 ? r32 : (r32 < 28 ? 20 : r32 - 24);
  const unsigned ka0 = hi * (AT_C * 16) + pk * 16, kb0 = AT_KB + pk * 8, v0 = AT_V + vrow * AT_RV + hi * 16;
  WAITV(10); BAR();
  f32x16 o = {}, nm, sA, sB;
  {
    bf16x8 ka = lds8(lds0 + ka0); s16x4 kb = *(const LAS s16x4*)(lds0 + kb0);
    sA = mfma32(ka, qa, f32x16{}); sA = mfma32k8(kb, qb4, sA);
    const float m = max16(sA);
#pragma unroll
    for (int r = 0; r < 16; ++r) { nm[r] = -m; sA[r] -= m; }
  }
  for (int ch = 0; ch < 4; ++ch) {
    const unsigned cur = lds0 + (ch % 3) * AT_SLOT, nxt = lds0 + ((ch + 1) % 3) * AT_SLOT;
#pragma unroll
    for (int t = 0; t < 16; t += 2) {
      if (t == 8 && ch < 3) { WAITV(0); BAR(); if (ch == 1) stage(3); }
      {
        bf16x8 ka = lds8(cur + ka0 + (t + 1) * 512); s16x4 kb = *(const LAS s16x4*)(cur + kb0 + (t + 1) * 256);
        bf16x8 va = lds8(cur + v0 + t * 64), vb = lds8(cur + v0 + t * 64 + 32);
        sB = mfma32(ka, qa, nm); sB = mfma32k8(kb, qb4, sB);
        exp16(sA);
        o = mfma32(va, pack8(sA, 0), o); o = mfma32(vb, pack8(sA, 8), o);
      }
      {
        const unsigned kbase = t + 2 < 16 ? cur : nxt; const int tn = (t + 2) & 15;
        bf16x8 ka = lds8(kbase + ka0 + tn * 512); s16x4 kb = *(const LAS s16x4*)(kbase + kb0 + tn * 256);
        bf16x8 va = lds8(cur + v0 + (t + 1) * 64), vb = lds8(cur + v0 + (t + 1) * 64 + 32);
        sA = mfma32(ka, qa, nm); sA = mfma32k8(kb, qb4, sA);
        exp16(sB);
        o = mfma32(va, pack8(sB, 0), o); o = mfma32(vb, pack8(sB, 8), o);
      }
    }
  }
  float l = __shfl(o[8], r32 + 32);
  float chk = l;
#pragma unroll
  for (int r = 0; r < 12; ++r) chk += __builtin_fabsf(hi && r >= 8 ? 0.f : o[r]);
  if (__builtin_expect(__any(!(chk < 3.0e38f)), 0)) {
    o = f32x16{};
    float m = -INFINITY;
    for (int t = 0; t < 64; ++t) {
      bf16x8 ka = ldg8(KA + (size_t)(hi * S + t * 32 + pk) * 16);
      s16x4 kb = *(const s16x4*)(KB + (size_t)(t * 32 + pk) * 8);
      const char* vp = VT + ((size_t)(r32 < 20 ? r32 : 0) * S + t * 32 + hi * 8) * 2;
      u32x4 va = *(const u32x4*)vp, vb = *(const u32x4*)(vp + 32);
      if (r32 >= 20) { va = u32x4{0x3f803f80u, 0x3f803f80u, 0x3f803f80u, 0x3f803f80u}; vb = va; }
      f32x16 s = mfma32(ka, qa, f32x16{}); s = mfma32k8(kb, qb4, s);
      const float mn = __builtin_fmaxf(m, max16(s));
      const float al = __builtin_amdgcn_exp2f(m - mn);
      m = mn;
#pragma unroll
      for (int r = 0; r < 16; ++r) { s[r] = __builtin_amdgcn_exp2f(s[r] - mn); o[r] *= al; }
      o = mfma32(__builtin_bit_cast(bf16x8, va), pack8(s, 0), o); o = mfma32(__builtin_bit_cast(bf16x8, vb), pack8(s, 8), o);
    }
    l = __shfl(o[8], r32 + 32);
  }
  const float rl = 1.0f / l;
  char* op = ws + WS_O + ((size_t)q * D + head * DH + 4 * hi) * 2;
  *(u32x2*)op = u32x2{pk2(o[0] * rl, o[1] * rl), pk2(o[2] * rl, o[3] * rl)};
  *(u32x2*)(op + 16) = u32x2{pk2(o[4] * rl, o[5] * rl), pk2(o[6] * rl, o[7] * rl)};
  if (!hi) *(u32x2*)(op + 32) = u32x2{pk2(o[8] * rl, o[9] * rl), pk2(o[10] * rl, o[11] * rl)};
}

template <int NKB>
__global__ __launch_bounds__(448) void k_proj(const char* __restrict__ A, const char* __restrict__ Wt, const u16* __restrict__ res,
                                              const u16* __restrict__ gate, u16* __restrict__ out) {
  extern __shared__ __attribute__((aligned(16))) char smem[];
  const int tid = threadIdx.x, lane = tid & 63, wv = ufl(tid >> 6);
  const int pi = (blockIdx.x & 7) * 4 + ((blockIdx.x >> 3) & 3), j = blockIdx.x >> 5;
  const unsigned lds0 = (unsigned)(uintptr_t)smem;
  constexpr int LDA = NKB * 128;
  if (wv >= 5) {
    const int lw = wv - 5, rr = lane >> 3, cc = lane & 7;
    const char* src = A + (size_t)(pi * 64 + lw * 32 + rr) * LDA + ((cc ^ rr) << 4);
#pragma unroll
    for (int kb = 0; kb < NKB; ++kb)
#pragma unroll
      for (int i = 0; i < 4; ++i) glds16(src + (size_t)i * 8 * LDA + kb * 128, lds0 + kb * 8192 + lw * 4096 + i * 1024);
#pragma unroll
    for (int gq = 0; gq < NKB / 2; ++gq) { WAITV((NKB / 2 - 1 - gq) * 8); BAR(); }
    return;
  }
  const int r = lane & 15, g = lane >> 4;
  const char* wp = Wt + (size_t)(j * 5 + wv) * (2 * NKB) * 1024 + lane * 16;
  bf16x8 bw[2 * NKB];
#pragma unroll
  for (int ks = 0; ks < 2 * NKB; ++ks) bw[ks] = ldg8(wp + ks * 1024);
  const int n0 = 80 * j + 16 * wv + 4 * g;
  u32x2 rv[4];
#pragma unroll
  for (int mt = 0; mt < 4; ++mt) rv[mt] = *(const u32x2*)(res + (size_t)(pi * 64 + mt * 16 + r) * D + n0);
  const u32x2 gv = *(const u32x2*)(gate + n0);
  const unsigned a0 = afrag_base(lds0, lane, 0), a1 = afrag_base(lds0, lane, 1);
  f32x4 acc[4] = {};
#pragma unroll
  for (int ks = 0; ks < 2 * NKB; ++ks) {
    if ((ks & 3) == 0) BAR();
    const unsigned ab = ((ks & 1) ? a1 : a0) + (ks >> 1) * 8192;
#pragma unroll
    for (int mt = 0; mt < 4; ++mt) acc[mt] = mfma16(bw[ks], lds8(ab + mt * 2048), acc[mt]);
  }
#pragma unroll
  for (int mt = 0; mt < 4; ++mt) {
    float y0 = bf_lo(rv[mt][0]) + bf_lo(gv[0]) * acc[mt][0], y1 = bf_hi(rv[mt][0]) + bf_hi(gv[0]) * acc[mt][1];
    float y2 = bf_lo(rv[mt][1]) + bf_lo(gv[1]) * acc[mt][2], y3 = bf_hi(rv[mt][1]) + bf_hi(gv[1]) * acc[mt][3];
    *(u32x2*)(out + (size_t)(pi * 64 + mt * 16 + r) * D + n0) = u32x2{pk2(y0, y1), pk2(y2, y3)};
  }
}

__global__ __launch_bounds__(512, 2) void k_win(char* __restrict__ ws) {
  extern __shared__ __attribute__((aligned(16))) char smem[];
  const int tid = threadIdx.x, lane = tid & 63, wv = ufl(tid >> 6);
  const int pi = (blockIdx.x & 7) * 4 + ((blockIdx.x >> 3) & 3), j = blockIdx.x >> 5;
  const unsigned lds0 = (unsigned)(uintptr_t)smem;
  LAS f32x2* part = (LAS f32x2*)(lds0 + 81920);
  const u16* mod = (const u16*)(ws + WS_MOD);
  LnRegs R;
  ln_load(R, (const u16*)(ws + WS_X1) + (size_t)pi * 64 * D, mod + 4 * D, mod + 3 * D, wv, lane);
  ln_store(R, lds0, wv, lane);
  BAR();
  const int r = lane & 15, g = lane >> 4;
  const unsigned a0 = afrag_base(lds0, lane, 0), a1 = afrag_base(lds0, lane, 1);
  f32x4 z2[4] = {}, z1[4] = {};
  GAS unsigned long long* gran = (GAS unsigned long long*)(ws + WS_STAT) + (size_t)pi * 512;
  if (wv < 7) {
    const char* wp = ws + WS_WIN + (size_t)(j * 14 + wv) * 20 * 1024 + lane * 16;
#pragma unroll 4
    for (int ks = 0; ks < 20; ++ks) {
      bf16x8 b = ldg8(wp + ks * 1024);
      const unsigned ab = ((ks & 1) ? a1 : a0) + (ks >> 1) * 8192;
#pragma unroll
      for (int mt = 0; mt < 4; ++mt) z2[mt] = mfma16(lds8(ab + mt * 2048), b, z2[mt]);
    }
#pragma unroll
    for (int mt = 0; mt < 4; ++mt)
#pragma unroll
      for (int e = 0; e < 4; ++e) {
        float z = rbf(gelu_tanh(z2[mt][e]));
        z2[mt][e] = z;
        float s1 = row16_sum(z), s2 = row16_sum(z * z);
        if (r == 0) part[wv * 64 + mt * 16 + 4 * g + e] = f32x2{s1, s2};
      }
  }
  BAR();
  if (wv == 7) {
    float s1 = 0.f, s2 = 0.f;
#pragma unroll
    for (int w = 0; w < 7; ++w) { f32x2 p = part[w * 64 + lane]; s1 += p[0]; s2 += p[1]; }
    unsigned b1 = __float_as_uint(s1), b2 = __float_as_uint(s2);
    if (b2 == ~0u) b2 = 0x7fc00000u;
    __hip_atomic_store(gran + j * 64 + lane, ((unsigned long long)b2 << 32) | b1, __ATOMIC_RELAXED, __HIP_MEMORY_SCOPE_AGENT);
    float t1, t2;
    for (unsigned spins = 0;; ++spins) {
      bool ok = true; t1 = 0.f; t2 = 0.f;
#pragma unroll
      for (int w = 0; w < 8; ++w) {
        unsigned long long v = __hip_atomic_load(gran + w * 64 + lane, __ATOMIC_RELAXED, __HIP_MEMORY_SCOPE_AGENT);
        ok &= (unsigned)(v >> 32) != ~0u;
        t1 += __uint_as_float((unsigned)v); t2 += __uint_as_float((unsigned)(v >> 32));
      }
      if (__all(ok) || spins > (1u << 20)) break;
      __builtin_amdgcn_s_sleep(2);
    }
    const float mu = t1 * (1.0f / FH);
    const float var = __builtin_fmaxf(t2 * (1.0f / FH) - mu * mu, 0.f);
    const float rs = __builtin_amdgcn_rsqf(var + 1e-5f);
    part[7 * 64 + lane] = f32x2{rs, -mu * rs};
  } else {
    const char* wp = ws + WS_WIN + (size_t)(j * 14 + 7 + wv) * 20 * 1024 + lane * 16;
#pragma unroll 4
    for (int ks = 0; ks < 20; ++ks) {
      bf16x8 b = ldg8(wp + ks * 1024);
      const unsigned ab = ((ks & 1) ? a1 : a0) + (ks >> 1) * 8192;
#pragma unroll
      for (int mt = 0; mt < 4; ++mt) z1[mt] = mfma16(b, lds8(ab + mt * 2048), z1[mt]);
    }
    char* zp = ws + WS_Z1 + (size_t)((pi * 8 + j) * 7 + wv) * 2048 + lane * 8;
#pragma unroll
    for (int mt = 0; mt < 4; ++mt)
      *(u32x2*)(zp + mt * 512) = u32x2{pk2(gelu_tanh(z1[mt][0]), gelu_tanh(z1[mt][1])), pk2(gelu_tanh(z1[mt][2]), gelu_tanh(z1[mt][3]))};
  }
  BAR();
  if (wv < 7) {
    char* zp = ws + WS_ZT + ((size_t)(j * 7 + wv) * 64 + pi * 2) * 1024 + ((g >> 1) * 16 + r) * 16 + (g & 1) * 8;
#pragma unroll
    for (int mt = 0; mt < 4; ++mt) {
      float y[4];
#pragma unroll
      for (int e = 0; e < 4; ++e) { f32x2 p = part[7 * 64 + mt * 16 + 4 * g + e]; y[e] = __builtin_fmaf(z2[mt][e], p[0], p[1]); }
      *(u32x2*)(zp + mt * 512) = u32x2{pk2(y[0], y[1]), pk2(y[2], y[3])};
    }
  }
}

constexpr int SP_RING = 16;
__global__ __launch_bounds__(512, 2) void k_sp(const u16* __restrict__ w_sp, const u16* __restrict__ b_sp, char* __restrict__ ws) {
  extern __shared__ __attribute__((aligned(16))) char smem[];
  const int tid = threadIdx.x, lane = tid & 63, wv = ufl(tid >> 6);
  const int pi = (blockIdx.x & 7) * 4 + ((blockIdx.x >> 3) & 3), j = blockIdx.x >> 5;
  const unsigned lds0 = (unsigned)(uintptr_t)smem;
  if (wv == 7) {
    const int rr = lane >> 3, cc = lane & 7;
    const char* src = (const char*)w_sp + (size_t)(pi * 64 + rr) * (S * 2) + ((cc ^ rr) << 4);
    auto grp = [&](int gq) {
#pragma unroll
      for (int h = 0; h < 2; ++h)
#pragma unroll
        for (int i = 0; i < 8; ++i)
          glds16(src + (size_t)i * 8 * (S * 2) + (gq * 2 + h) * 128, lds0 + ((gq * 2 + h) & (SP_RING - 1)) * 8192 + i * 1024);
    };
    grp(0); grp(1); grp(2);
    for (int gq = 0; gq < 13; ++gq) { WAITV(32); BAR(); grp(gq + 3); }
    WAITV(32); BAR(); WAITV(16); BAR(); WAITV(0); BAR();
    return;
  }
  const int r = lane & 15, g = lane >> 4;
  const char* zp = ws + WS_ZT + (size_t)(j * 7 + wv) * 64 * 1024 + lane * 16;
  const unsigned a0 = afrag_base(lds0, lane, 0), a1 = afrag_base(lds0, lane, 1);
  constexpr int WIN = 16;
  bf16x8 bw[WIN];
#pragma unroll
  for (int ks = 0; ks < WIN; ++ks) bw[ks] = ldg8(zp + ks * 1024);
  u32x2 z1v[4]; float bs[4];
#pragma unroll
  for (int mt = 0; mt < 4; ++mt) {
    z1v[mt] = *(const u32x2*)(ws + WS_Z1 + (size_t)((pi * 8 + j) * 7 + wv) * 2048 + lane * 8 + mt * 512);
    bs[mt] = bf2f(b_sp[pi * 64 + mt * 16 + r]);
  }
  f32x4 acc[4] = {};
  for (int k0 = 0; k0 < 64; k0 += WIN) {
    const int kn = k0 + WIN < 64 ? k0 + WIN : k0;
    const unsigned rb = ((k0 >> 1) & (SP_RING - 1)) * 8192;
#pragma unroll
    for (int ks = 0; ks < WIN; ++ks) {
      if ((ks & 3) == 0) BAR();
      const unsigned ab = ((ks & 1) ? a1 : a0) + rb + (ks >> 1) * 8192;
#pragma unroll
      for (int mt = 0; mt < 4; ++mt) acc[mt] = mfma16(bw[ks], lds8(ab + mt * 2048), acc[mt]);
      bw[ks] = ldg8(zp + (kn + ks) * 1024);
    }
  }
  u16* up = (u16*)(ws + WS_U) + (size_t)(pi * 64 + r) * UP + 112 * j + 16 * wv + 4 * g;
#pragma unroll
  for (int mt = 0; mt < 4; ++mt) {
    float y0 = bf_lo(z1v[mt][0]) * (acc[mt][0] + bs[mt]), y1 = bf_hi(z1v[mt][0]) * (acc[mt][1] + bs[mt]);
    float y2 = bf_lo(z1v[mt][1]) * (acc[mt][2] + bs[mt]), y3 = bf_hi(z1v[mt][1]) * (acc[mt][3] + bs[mt]);
    *(u32x2*)(up + (size_t)mt * 16 * UP) = u32x2{pk2(y0, y1), pk2(y2, y3)};
  }
}

extern "C" void kernel_launch(void* const* d_in, const int* in_sizes, int n_in, void* d_out, int out_size, void* d_ws,
                              size_t ws_size, hipStream_t stream) {
  const u16* x = (const u16*)d_in[0];
  const u16* wqkv = (const u16*)d_in[1];
  const u16* wo = (const u16*)d_in[2];
  const u16* w_in = (const u16*)d_in[4];
  const u16* w_sp = (const u16*)d_in[5];
  const u16* b_sp = (const u16*)d_in[6];
  const u16* w_out = (const u16*)d_in[7];
  const u16* c = (const u16*)d_in[8];
  const u16* w_mod = (const u16*)d_in[9];
  char* ws = (char*)d_ws;
  const u16* mod = (const u16*)(ws + WS_MOD);
  (void)hipFuncSetAttribute((const void*)k_qkv, hipFuncAttributeMaxDynamicSharedMemorySize, 81920);
  (void)hipFuncSetAttribute((const void*)k_attn, hipFuncAttributeMaxDynamicSharedMemorySize, AT_LDS);
  (void)hipFuncSetAttribute((const void*)k_proj<10>, hipFuncAttributeMaxDynamicSharedMemorySize, 10 * 8192);
  (void)hipFuncSetAttribute((const void*)k_proj<14>, hipFuncAttributeMaxDynamicSharedMemorySize, 14 * 8192);
  (void)hipFuncSetAttribute((const void*)k_win, hipFuncAttributeMaxDynamicSharedMemorySize, 81920 + 4096);
  (void)hipFuncSetAttribute((const void*)k_sp, hipFuncAttributeMaxDynamicSharedMemorySize, SP_RING * 8192);
  k_prep<<<256 + 1640, 256, 0, stream>>>(c, w_mod, wqkv, wo, w_in, w_out, ws);
  k_qkv<<<256, 512, 81920, stream>>>(x, ws);
  k_attn<<<256, 512, AT_LDS, stream>>>(ws);
  k_proj<10><<<256, 448, 10 * 8192, stream>>>(ws + WS_O, ws + WS_WO, x, mod + 2 * D, (u16*)(ws + WS_X1));
  k_win<<<256, 512, 81920 + 4096, stream>>>(ws);
  k_sp<<<256, 512, SP_RING * 8192, stream>>>(w_sp, b_sp, ws);
  k_proj<14><<<256, 448, 14 * 8192, stream>>>(ws + WS_U, ws + WS_WOUT, (const u16*)(ws + WS_X1), mod + 5 * D, (u16*)d_out);
}
```

```cpp
#if defined(__HIP_DEVICE_COMPILE__)
#pragma clang attribute push(__attribute__((target("no-packed-fp32-ops"))), apply_to = function)
#endif
#include <hip/hip_runtime.h>
#include <hip/hip_bf16.h>
#include <stdint.h>

typedef unsigned short u16;
typedef __attribute__((ext_vector_type(8))) __bf16 bf16v8;
typedef __attribute__((ext_vector_type(2))) __bf16 bf16x2;
typedef __attribute__((ext_vector_type(4))) short s16x4;
typedef __attribute__((ext_vector_type(2))) float f32x2;
typedef __attribute__((ext_vector_type(4))) float f32x4;
typedef __attribute__((ext_vector_type(16))) float f32x16;
typedef __attribute__((ext_vector_type(4))) unsigned u32x4;
typedef __attribute__((ext_vector_type(2))) unsigned u32x2;
typedef u32x4 frag;
#define LAS __attribute__((address_space(3)))
#define GAS __attribute__((address_space(1)))
#define DEVI __device__ __forceinline__
#define TID ((int)__builtin_amdgcn_workitem_id_x())
#define BID ((int)__builtin_amdgcn_workgroup_id_x())

constexpr int S = 2048, D = 640, NH = 32, DH = 20, FF = 1728, FH = 864;
constexpr int NV = 22;

constexpr int WS_MOD  = 0;
constexpr int WS_UV   = 16384;
constexpr int WS_STAT = 32768;
constexpr int WS_XST  = WS_STAT + 32 * 8 * 64 * 8;
constexpr int WS_UVP  = WS_XST + 32 * 8 * 64 * 8;
constexpr int WS_WQKV = WS_UVP + 40 * 1792 * 8;
constexpr int WS_WO   = WS_WQKV + 8 * 15 * 20 * 1024;
constexpr int WS_WIN  = WS_WO + 40 * 20 * 1024;
constexpr int WS_WOUT = WS_WIN + 8 * 14 * 20 * 1024;
constexpr int WS_QA   = WS_WOUT + 40 * 28 * 1024;
constexpr int WS_QB   = WS_QA + NH * S * 32;
constexpr int WS_KA   = WS_QB + NH * S * 8;
constexpr int WS_KB   = WS_KA + NH * S * 32;
constexpr int WS_VT   = WS_KB + NH * S * 8;
constexpr int WS_O    = WS_VT + NH * 256 * NV * 16;
constexpr int WS_X1   = WS_O + S * D * 2;
constexpr int WS_ZT   = WS_X1 + S * D * 2;
constexpr int WS_Z1   = WS_ZT + 8 * 7 * 64 * 1024;
constexpr int WS_U    = WS_Z1 + 32 * 8 * 7 * 2 * 1024;
constexpr int WS_END  = WS_U + 32 * 28 * 4 * 1024;

#define TS_DECL
#define TS_ARGS
#define TS_PASS
#define STAMP(s)
#define STAMP_END(ws, k, s)

DEVI float bf_lo(unsigned w) { return __uint_as_float(w << 16); }
DEVI float bf_hi(unsigned w) { return __uint_as_float(w & 0xffff0000u); }
DEVI float bf2f(u16 h) { return __uint_as_float((unsigned)h << 16); }
DEVI unsigned pk2(float a, float b) { bf16x2 v = {(__bf16)a, (__bf16)b}; return __builtin_bit_cast(unsigned, v); }
DEVI float rbf(float a) { return bf_lo(pk2(a, a)); }
DEVI u32x2 pk4(const f32x4& a) { return u32x2{pk2(a[0], a[1]), pk2(a[2], a[3])}; }
DEVI u32x4 pk8(const f32x4& a, const f32x4& b) { return u32x4{pk2(a[0], a[1]), pk2(a[2], a[3]), pk2(b[0], b[1]), pk2(b[2], b[3])}; }
DEVI f32x4 up4(unsigned a, unsigned b) { return f32x4{bf_lo(a), bf_hi(a), bf_lo(b), bf_hi(b)}; }
DEVI float dot2(unsigned a, unsigned b, float c) { return __builtin_amdgcn_fdot2_f32_bf16(__builtin_bit_cast(bf16x2, a), __builtin_bit_cast(bf16x2, b), c, false); }
DEVI f32x4 mfma16(frag a, frag b, f32x4 c) { return __builtin_amdgcn_mfma_f32_16x16x32_bf16(__builtin_bit_cast(bf16v8, a), __builtin_bit_cast(bf16v8, b), c, 0, 0, 0); }
DEVI f32x16 mfma32(frag a, frag b, f32x16 c) { return __builtin_amdgcn_mfma_f32_32x32x16_bf16(__builtin_bit_cast(bf16v8, a), __builtin_bit_cast(bf16v8, b), c, 0, 0, 0); }
DEVI f32x16 mfma32k8(u32x2 a, u32x2 b, f32x16 c) { return __builtin_amdgcn_mfma_f32_32x32x8bf16_1k(__builtin_bit_cast(s16x4, a), __builtin_bit_cast(s16x4, b), c, 0, 0, 0); }
DEVI frag lds8(unsigned a) { return *(const LAS frag*)a; }
DEVI u32x2 lds4(unsigned a) { return *(const LAS u32x2*)a; }
typedef __amdgpu_buffer_rsrc_t rsrc_t;
DEVI rsrc_t mk_rsrc(const void* p, int bytes) { return __builtin_amdgcn_make_buffer_rsrc((void*)p, 0, bytes, 0x00020000); }
DEVI frag bld16(rsrc_t r, int voff, int soff) { return __builtin_amdgcn_raw_buffer_load_b128(r, voff, soff, 0); }
DEVI u32x2 bld8(rsrc_t r, int voff, int soff) { return __builtin_amdgcn_raw_buffer_load_b64(r, voff, soff, 0); }
#define ST_AUX 0
DEVI void bst16(rsrc_t r, int voff, int soff, u32x4 v) { __builtin_amdgcn_raw_buffer_store_b128(v, r, voff + soff, 0, ST_AUX); }
DEVI void bst8(rsrc_t r, int voff, int soff, u32x2 v) { __builtin_amdgcn_raw_buffer_store_b64(v, r, voff, soff, ST_AUX); }
DEVI void glds16(const void* base, unsigned voff, unsigned lds_base) {
  unsigned sv;
  asm volatile("s_mov_b32 %0, m0\n\ts_mov_b32 m0, %3\n\ts_nop 0\n\tglobal_load_lds_dwordx4 %1, %2\n\ts_mov_b32 m0, %0"
               : "=&s"(sv) : "v"(voff), "s"(base), "s"(lds_base) : "memory");
}
#define WAITV(n) asm volatile("s_waitcnt vmcnt(%0)" ::"i"(n) : "memory")
#define BAR() asm volatile("s_waitcnt lgkmcnt(0)\n\ts_barrier" ::: "memory")
#define RAWBAR() asm volatile("s_barrier" ::: "memory")
#define SBAR() __builtin_amdgcn_sched_barrier(0)
#define PIN(x) asm volatile("" : "+v"(x))
DEVI int ufl(int x) { return __builtin_amdgcn_readfirstlane(x); }
template <int CTRL> DEVI float dpp_add(float v) {
  return v + __builtin_bit_cast(float, __builtin_amdgcn_update_dpp(0, __builtin_bit_cast(int, v), CTRL, 0xf, 0xf, true));
}
DEVI float row16_sum(float v) {
  v = dpp_add<0x128>(v); v = dpp_add<0x124>(v); v = dpp_add<0x122>(v); v = dpp_add<0x121>(v);
  return v;
}
DEVI float quad4_sum(const f32x4& v) {
  float x, y, o;
  asm("s_nop 1\n\t"
      "v_add_f32_dpp %0, %3, %3 row_ror:8 row_mask:0xf bank_mask:0x3\n\t"
      "v_add_f32_dpp %1, %4, %4 row_ror:8 row_mask:0xf bank_mask:0x3\n\t"
      "v_add_f32_dpp %0, %5, %5 row_ror:8 row_mask:0xf bank_mask:0xc\n\t"
      "v_add_f32_dpp %1, %6, %6 row_ror:8 row_mask:0xf bank_mask:0xc\n\t"
      "s_nop 0\n\t"
      "v_add_f32_dpp %2, %0, %0 row_half_mirror row_mask:0xf bank_mask:0x5\n\t"
      "v_add_f32_dpp %2, %1, %1 row_half_mirror row_mask:0xf bank_mask:0xa\n\t"
      "s_nop 1\n\t"
      "v_add_f32_dpp %2, %2, %2 quad_perm:[1,0,3,2] row_mask:0xf bank_mask:0xf\n\t"
      "s_nop 1\n\t"
      "v_add_f32_dpp %2, %2, %2 quad_perm:[2,3,0,1] row_mask:0xf bank_mask:0xf"
      : "=&v"(x), "=&v"(y), "=&v"(o) : "v"(v[0]), "v"(v[1]), "v"(v[2]), "v"(v[3]));
  return o;
}
DEVI float gelu_tanh(float x) {
  const float c1 = -2.0f * 0.7978845608028654f * 1.4426950408889634f, c3 = c1 * 0.044715f;
  float e = __builtin_amdgcn_exp2f(x * __builtin_fmaf(x * x, c3, c1));
  return x * __builtin_amdgcn_rcpf(1.0f + e);
}
DEVI int prow(int mt, int r) { return 32 * (mt >> 1) + 8 * (r >> 2) + 4 * (mt & 1) + (r & 3); }
DEVI int pinv(int o) { return (o & ~31) | ((o & 4) << 2) | ((o >> 1) & 12) | (o & 3); }

DEVI void rows8(unsigned (&r)[8], const u16* __restrict__ col, int ldw, int k0, int k1, bool ok0, bool ok1) {
#pragma unroll
  for (int i = 0; i < 8; ++i)
    r[i] = (i < 4 ? ok0 : ok1) ? __builtin_nontemporal_load((const unsigned*)(col + (size_t)((i < 4 ? k0 : k1 - 4) + i) * ldw)) : 0u;
}
DEVI void put2(char* dst, const unsigned (&r)[8]) {
  u32x4 c0, c1;
#pragma unroll
  for (int i = 0; i < 4; ++i) {
    c0[i] = __builtin_amdgcn_perm(r[2 * i + 1], r[2 * i], 0x05040100u);
    c1[i] = __builtin_amdgcn_perm(r[2 * i + 1], r[2 * i], 0x07060302u);
  }
  ((u32x4*)dst)[0] = c0; ((u32x4*)dst)[1] = c1;
}
DEVI int ncol80(int n) { const int j = n / 80, o = n - j * 80; return j * 80 + (o < 64 ? pinv(o) : o); }
template <int W, int NP> DEVI void krows(int ks, int g, int& k0, int& k1, bool& ok) {
  ok = true;
  if (ks < 8 * NP) { const int j = ks / NP; k0 = W * j + 32 * (ks - j * NP) + 8 * g; k1 = k0 + 4; }
  else { k0 = W * 2 * (ks - 8 * NP) + 32 * NP + 4 * g; k1 = k0 + W; ok = 32 * NP + 4 * g < W; }
}
DEVI void tile_wo(const u16* __restrict__ wo, char* ws, int f, int lane) {
  const int k8 = f / 5, n = (f - k8 * 5) * 128 + 2 * lane;
  int k0, k1;
  if (k8 < 64) { k0 = DH * (k8 >> 1) + 4 * (k8 & 1); k1 = k0 + 8; } else { k0 = 2 * DH * (k8 - 64) + 16; k1 = k0 + DH; }
  unsigned r[8]; rows8(r, wo + n, D, k0, k1, true, true);
  const int c = ncol80(n);
  put2(ws + WS_WO + (((c / 80) * 20 + (k8 >> 2)) * 5 + (c % 80 >> 4)) * 1024 + ((k8 & 3) * 16 + (c & 15)) * 16, r);
}
DEVI void tile_wout(const u16* __restrict__ w_out, char* ws, int f, int lane) {
  const int k8 = f / 5, n = (f - k8 * 5) * 128 + 2 * lane;
  int k0, k1; bool ok; krows<108, 3>(k8 >> 2, k8 & 3, k0, k1, ok);
  unsigned r[8]; rows8(r, w_out + n, D, k0, k1, ok, ok);
  const int c = ncol80(n);
  put2(ws + WS_WOUT + (((c / 80) * 28 + (k8 >> 2)) * 5 + (c % 80 >> 4)) * 1024 + ((k8 & 3) * 16 + (c & 15)) * 16, r);
}

constexpr int PT_QKV = 80 * 15, PT_WO = 80 * 5, PT_WOUT = 112 * 5;
constexpr int PT_BLOCKS = (PT_QKV + PT_WO + PT_WOUT) / 4;
__global__ __launch_bounds__(256) void k_prep(const u16* __restrict__ c, const u16* __restrict__ w_mod, const u16* __restrict__ wqkv,
                                              const u16* __restrict__ wo, const u16* __restrict__ w_out, char* __restrict__ ws) {
  __shared__ float red[4][16];
  const int tid = TID, lane = tid & 63, wv = tid >> 6, b = BID;
  TS_DECL; STAMP(0);
  if (b < 240) {
    const int cb = (b & 7) * 30 + (b >> 3);
    const int half = tid & 1, slot = tid >> 1;
    u32x4 v[5]; float sc[5];
#pragma unroll
    for (int i = 0; i < 5; ++i) {
      int k = slot + 128 * i;
      v[i] = __builtin_nontemporal_load((const u32x4*)(w_mod + (size_t)k * (6 * D) + cb * 16 + half * 8));
      float x = bf2f(c[k]);
      sc[i] = rbf(x * __builtin_amdgcn_rcpf(1.0f + __builtin_amdgcn_exp2f(-1.4426950408889634f * x)));
    }
    float a[8];
#pragma unroll
    for (int j = 0; j < 8; ++j) a[j] = 0.f;
#pragma unroll
    for (int i = 0; i < 5; ++i)
#pragma unroll
      for (int j = 0; j < 4; ++j) { a[2 * j] += sc[i] * bf_lo(v[i][j]); a[2 * j + 1] += sc[i] * bf_hi(v[i][j]); }
#pragma unroll
    for (int j = 0; j < 8; ++j) {
      a[j] = dpp_add<0x128>(a[j]); a[j] = dpp_add<0x124>(a[j]); a[j] = dpp_add<0x122>(a[j]);
      a[j] += __shfl_xor(a[j], 16); a[j] += __shfl_xor(a[j], 32);
    }
    if (lane < 2) {
#pragma unroll
      for (int j = 0; j < 8; ++j) red[wv][lane * 8 + j] = a[j];
    }
    __syncthreads();
    if (tid < 16) {
      float s = (red[0][tid] + red[1][tid]) + (red[2][tid] + red[3][tid]);
      int col = cb * 16 + tid, seg = col / D;
      s = rbf(s);
      if (seg == 1 || seg == 4) s = rbf(1.0f + s);
      ((float*)(ws + WS_MOD))[col] = s;
    }
    STAMP_END(ws, 0, 1);
    return;
  }
  if (b < 256) {
    u32x4* p = (u32x4*)(ws + WS_STAT) + (b - 240) * 512;
    p[tid] = u32x4{~0u, ~0u, ~0u, ~0u}; p[tid + 256] = u32x4{~0u, ~0u, ~0u, ~0u};
    return;
  }
  int f = (b - 256) * 4 + wv;
  if (f < PT_QKV) {
    const int k8 = f / 15, n = (f - k8 * 15) * 128 + 2 * lane;
    unsigned r[8]; rows8(r, wqkv + n, 3 * D, k8 * 8, k8 * 8 + 4, true, true);
    const int seg = n / D, cc = n - seg * D, j = cc / 80, o = cc - j * 80, hh = o / DH, d = o - hh * DH;
    int tile, rho;
    if (seg == 2) { tile = 10 + (o >> 4); rho = o & 15; }
    else if (d < 16) { const int e8 = 2 * hh + (d >> 3); tile = seg * 4 + 2 * (e8 >> 2) + ((d >> 2) & 1); rho = 4 * (e8 & 3) + (d & 3); }
    else { tile = 8 + seg; rho = 4 * hh + d - 16; }
    put2(ws + WS_WQKV + ((j * 15 + tile) * 20 + (k8 >> 2)) * 1024 + ((k8 & 3) * 16 + rho) * 16, r);
    STAMP_END(ws, 0, 2);
    return;
  }
  f -= PT_QKV;
  if (f < PT_WO) { tile_wo(wo, ws, f, lane); return; }
  tile_wout(w_out, ws, f - PT_WO, lane);
  STAMP_END(ws, 0, 3);
}

constexpr int Q_GS = 81920, Q_LDS = Q_GS + 4 * 5120;
DEVI unsigned afrag_base(unsigned lds0, int lane, int odd) {
  const int r = lane & 15, g = lane >> 4;
  return lds0 + r * 128 + (((4 * odd + g) ^ (r & 7)) << 4);
}
constexpr int Q_AHEAD = 8;
template <bool VW>
DEVI void qkv_mma(char* __restrict__ ws, unsigned lds0, int wv, int pi, int j) {
  const int lane = TID & 63, r = lane & 15, g = lane >> 4;
  TS_DECL;
  const rsrc_t wr = mk_rsrc(ws + WS_WQKV + (j * 15 + 2 * wv) * 20 * 1024, wv < 7 ? 40 * 1024 : 20 * 1024);
  frag bw[20][2], a[2][4];
  auto ldb = [&](int ks) { if (ks < 20) { bw[ks][0] = bld16(wr, lane * 16, ks * 1024); bw[ks][1] = bld16(wr, lane * 16, (20 + ks) * 1024); } };
#pragma unroll
  for (int ks = 0; ks < Q_AHEAD; ++ks) ldb(ks);
  SBAR();
  f32x4 acc[2][4] = {};
  const unsigned a0 = afrag_base(lds0, lane, 0), a1 = afrag_base(lds0, lane, 1);
  STAMP(4);
  RAWBAR();
  STAMP(1);
#pragma unroll
  for (int mt = 0; mt < 4; ++mt) a[0][mt] = lds8(a0 + mt * 2048);
#pragma unroll
  for (int ks = 0; ks < 20; ++ks) {
    const int kl = ks + 1;
    if (kl < 20) {
      if ((kl & 3) == 0) RAWBAR();
#pragma unroll
      for (int mt = 0; mt < 4; ++mt) a[kl & 1][mt] = lds8(((kl & 1) ? a1 : a0) + (kl >> 1) * 8192 + mt * 2048);
    }
    SBAR();
#pragma unroll
    for (int mt = 0; mt < 4; ++mt)
#pragma unroll
      for (int t = 0; t < 2; ++t)
        acc[t][mt] = VW ? mfma16(a[ks & 1][mt], bw[ks][t], acc[t][mt]) : mfma16(bw[ks][t], a[ks & 1][mt], acc[t][mt]);
    SBAR();
    ldb(ks + Q_AHEAD);
    SBAR();
  }
  STAMP(5);
  const rsrc_t wsr = mk_rsrc(ws, WS_END);
  if (!VW) {
    const float qs = 0.22360679774997896f * 1.4426950408889634f;
    const int s0 = pi * 64 + prow(0, r);
    if (wv < 4) {
      const float sc = wv < 2 ? qs : 1.0f;
      const int voff = (8 * j + 4 * (wv & 1) + g) * (S * 16) + s0 * 16;
#pragma unroll
      for (int mt = 0; mt < 4; ++mt) bst16(wsr, voff, (wv < 2 ? WS_QA : WS_KA) + prow(mt, 0) * 16, pk8(acc[0][mt] * sc, acc[1][mt] * sc));
    } else {
      const int voff = (4 * j + g) * (S * 8) + s0 * 8;
#pragma unroll
      for (int mt = 0; mt < 4; ++mt) {
        bst8(wsr, voff, WS_QB + prow(mt, 0) * 8, pk4(acc[0][mt] * qs));
        bst8(wsr, voff, WS_KB + prow(mt, 0) * 8, pk4(acc[1][mt]));
      }
    }
  } else {
#pragma unroll
    for (int t = 0; t < 2; ++t) {
      if (t == 1 && wv == 7) break;
      const int o = 16 * (2 * wv + t - 10) + r, hh = o / DH, d = o - hh * DH;
      const int voff = (((4 * j + hh) * 256 + pi * 8 + g) * NV + d) * 16;
      bst16(wsr, voff, WS_VT, pk8(acc[t][0], acc[t][1]));
      bst16(wsr, voff, WS_VT + 4 * NV * 16, pk8(acc[t][2], acc[t][3]));
    }
  }
  STAMP(2);
  STAMP_END(ws, 1, 3);
}
__global__ __launch_bounds__(768) void k_qkv(const u16* __restrict__ x, char* __restrict__ ws) {
  extern __shared__ __attribute__((aligned(16))) char smem[];
  const int wv = ufl(TID >> 6), lane = TID & 63;
  const int pi = ((BID & 7) >> 1) * 8 + ((BID >> 3) & 7), j = (BID & 1) * 4 + (BID >> 6);
  const unsigned lds0 = (unsigned)(uintptr_t)smem;
  if (wv >= 9) { qkv_mma<true>(ws, lds0, wv - 4, pi, j); return; }
  if (wv >= 4) { qkv_mma<false>(ws, lds0, wv - 4, pi, j); return; }
  TS_DECL; STAMP(0);
  const int r = lane & 15, g = lane >> 4;
  u32x4 xv[4][5];
  {
    const rsrc_t xr = mk_rsrc(x + (size_t)(pi * 64 + wv * 16) * D, 16 * D * 2);
#pragma unroll
    for (int p = 0; p < 4; ++p)
#pragma unroll
      for (int i = 0; i < 5; ++i) xv[p][i] = bld16(xr, g * (D * 2) + r * 16, p * 4 * D * 2 + i * 256);
  }
#pragma unroll
  for (int i = 0; i < 5; ++i) glds16(ws + WS_MOD + i * 1024, lane * 16, lds0 + Q_GS + wv * 5120 + i * 1024);
  if (wv == 1) {
    const unsigned cv = (lane & 1) ? 0u : 0x3f803f80u;
    bst16(mk_rsrc(ws, WS_END), (((4 * j + g) * 256 + pi * 8 + (r >> 1)) * NV + DH + (lane & 1)) * 16, WS_VT, u32x4{cv, cv, cv, cv});
  }
  SBAR();
  STAMP(6);
  const unsigned gsa = lds0 + Q_GS + wv * 5120 + r * 32;
  f32x4 gs[2][4];
  auto ldgs = [&](int i) {
    gs[i & 1][0] = *(const LAS f32x4*)(gsa + i * 512); gs[i & 1][1] = *(const LAS f32x4*)(gsa + i * 512 + 16);
    gs[i & 1][2] = *(const LAS f32x4*)(gsa + 2560 + i * 512); gs[i & 1][3] = *(const LAS f32x4*)(gsa + 2560 + i * 512 + 16);
  };
  float rs[4], nb[4];
#pragma unroll
  for (int p = 0; p < 4; ++p) {
    float s1 = 0.f, s2 = 0.f;
#pragma unroll
    for (int i = 0; i < 5; ++i)
#pragma unroll
      for (int e = 0; e < 4; ++e) { const unsigned w = xv[p][i][e]; s1 = dot2(w, 0x3f803f80u, s1); s2 = dot2(w, w, s2); }
    const float mu = row16_sum(s1) * (1.0f / D);
    const float var = __builtin_fmaxf(row16_sum(s2) * (1.0f / D) - mu * mu, 0.f);
    rs[p] = __builtin_amdgcn_rsqf(var + 1e-5f);
    nb[p] = -mu * rs[p];
    PIN(rs[p]); PIN(nb[p]);
    if (p == 2) { WAITV(0); ldgs(0); }
  }
  STAMP(7);
  unsigned wa[2];
#pragma unroll
  for (int h = 0; h < 2; ++h) {
    const int ri = 8 * (wv & 1) + 4 * h + g;
    wa[h] = lds0 + (r >> 3) * 8192 + ((wv >> 1) * 32 + ri) * 128 + (((r & 7) ^ (ri & 7)) << 4);
  }
#pragma unroll
  for (int i = 0; i < 5; ++i) {
    if (i < 4) ldgs(i + 1);
#pragma unroll
    for (int p = 0; p < 4; ++p) {
      u32x4 o;
      PIN(xv[p][i]);
#pragma unroll
      for (int e = 0; e < 4; ++e) {
        const unsigned w = xv[p][i][e];
        const f32x4 &sv = gs[i & 1][e >> 1], &gv = gs[i & 1][2 + (e >> 1)];
        o[e] = pk2(__builtin_fmaf(__builtin_fmaf(bf_lo(w), rs[p], nb[p]), gv[2 * (e & 1)], sv[2 * (e & 1)]),
                   __builtin_fmaf(__builtin_fmaf(bf_hi(w), rs[p], nb[p]), gv[2 * (e & 1) + 1], sv[2 * (e & 1) + 1]));
      }
      *(LAS u32x4*)(wa[p >> 1] + i * 16384 + (p & 1) * 2048) = o;
    }
    BAR();
  }
  STAMP_END(ws, 1, 8);
}

constexpr int AT_C = 512;
constexpr int AT_KB = 2 * AT_C * 16, AT_V = AT_KB + AT_C * 8, AT_SLOT = AT_V + (AT_C / 8) * NV * 16;
constexpr int AT_RED = 3 * AT_SLOT, AT_LDS = AT_RED + 8 * 3072;
DEVI float max16(const f32x16& p) {
  float a = __builtin_fmaxf(__builtin_fmaxf(p[0], p[1]), p[2]), b = __builtin_fmaxf(__builtin_fmaxf(p[3], p[4]), p[5]);
  a = __builtin_fmaxf(__builtin_fmaxf(a, p[6]), p[7]); b = __builtin_fmaxf(__builtin_fmaxf(b, p[8]), p[9]);
  a = __builtin_fmaxf(__builtin_fmaxf(a, p[10]), p[11]); b = __builtin_fmaxf(__builtin_fmaxf(b, p[12]), p[13]);
  a = __builtin_fmaxf(__builtin_fmaxf(a, p[14]), p[15]);
  float m = __builtin_fmaxf(a, b);
  auto rr = __builtin_amdgcn_permlane32_swap(__float_as_uint(m), __float_as_uint(m), false, false);
  return __builtin_fmaxf(__uint_as_float(rr[0]), __uint_as_float(rr[1]));
}
DEVI frag pack8(const f32x16& p, int o) {
  return frag{pk2(p[o], p[o + 1]), pk2(p[o + 2], p[o + 3]), pk2(p[o + 4], p[o + 5]), pk2(p[o + 6], p[o + 7])};
}
DEVI void tile_win(const u16* __restrict__ w_in, char* ws, int f, int lane) {
  const int k16 = f / 14, n = (f - k16 * 14) * 128 + 2 * lane;
  const bool real = n < FF;
  int np;
  if (real) { const int seg = n < FH, nn = n - (seg ? 0 : FH), j = nn / 108, o = nn - j * 108; np = (2 * j + seg) * 112 + (o < 96 ? pinv(o) : o); }
  else { const int e = (n - FF) >> 1; np = (e >> 1) * 112 + 108 + 2 * (e & 1); }
  const float* mod = (const float*)(ws + WS_MOD);
  unsigned r[2][8]; int k0[2], k1[2];
#pragma unroll
  for (int h = 0; h < 2; ++h) { bool ok; krows<80, 2>(k16 >> 1, 2 * (k16 & 1) + h, k0[h], k1[h], ok); rows8(r[h], w_in + n, FF, k0[h], k1[h], real, real); }
  RAWBAR();
  float u0 = 0.f, u1 = 0.f, v0 = 0.f, v1 = 0.f;
#pragma unroll
  for (int h = 0; h < 2; ++h) {
    u32x4 c0, c1;
#pragma unroll
    for (int i = 0; i < 8; i += 2) {
      const int ka = (i < 4 ? k0[h] : k1[h] - 4) + i;
      const float ga = mod[4 * D + ka], gb = mod[4 * D + ka + 1], sa = mod[3 * D + ka], sb = mod[3 * D + ka + 1];
      const float a0 = bf_lo(r[h][i]), a1 = bf_hi(r[h][i]), b0 = bf_lo(r[h][i + 1]), b1 = bf_hi(r[h][i + 1]);
      const unsigned w0 = pk2(a0 * ga, b0 * gb), w1 = pk2(a1 * ga, b1 * gb);
      c0[i >> 1] = w0; c1[i >> 1] = w1;
      u0 = dot2(w0, 0x3f803f80u, u0); u1 = dot2(w1, 0x3f803f80u, u1);
      v0 = __builtin_fmaf(a0, sa, __builtin_fmaf(b0, sb, v0)); v1 = __builtin_fmaf(a1, sa, __builtin_fmaf(b1, sb, v1));
    }
    u32x4* dst = (u32x4*)(ws + WS_WIN + ((np >> 4) * 20 + (k16 >> 1)) * 1024 + ((2 * (k16 & 1) + h) * 16 + (np & 15)) * 16);
    dst[0] = c0; dst[1] = c1;
  }
  *(f32x4*)(ws + WS_UVP + (k16 * 1792 + np) * 8) = f32x4{u0, v0, u1, v1};
}
__global__ __launch_bounds__(768) void k_attn(const u16* __restrict__ w_in, char* __restrict__ ws) {
  extern __shared__ __attribute__((aligned(16))) char smem[];
  const int tid = TID, lane = tid & 63, wv = ufl(tid >> 6), r32 = lane & 31, hi = lane >> 5;
  TS_DECL; STAMP(0);
  if (wv >= 8) {
    __builtin_amdgcn_s_setprio(3);
    const int f = BID * 4 + wv - 8;
    if (f < 560) tile_win(w_in, ws, f, lane);
    STAMP_END(ws, 2, 4);
    return;
  }
  const int head = (BID & 7) * 4 + ((BID >> 3) & 3), qb = BID >> 5, qg = wv & 3, kh = wv >> 2;
  const unsigned lds0 = (unsigned)(uintptr_t)smem;
  const char* KA = ws + WS_KA + head * (S * 32);
  const char* KB = ws + WS_KB + head * (S * 8);
  const char* VT = ws + WS_VT + head * (256 * NV * 16);
  auto stage = [&](int ch) {
    const unsigned sl = lds0 + (ch % 3) * AT_SLOT;
    glds16(KA + ch * (AT_C * 16) + wv * 1024, lane * 16, sl + wv * 1024);
    glds16(KA + (S + ch * AT_C) * 16 + wv * 1024, lane * 16, sl + AT_C * 16 + wv * 1024);
    if (wv < 4) glds16(KB + ch * (AT_C * 8) + wv * 1024, lane * 16, sl + AT_KB + wv * 1024);
    glds16(VT + ch * (NV * 1024) + wv * 1024, lane * 16, sl + AT_V + wv * 1024);
    glds16(VT + ch * (NV * 1024) + (8 + wv) * 1024, lane * 16, sl + AT_V + (8 + wv) * 1024);
    if (wv >= 2) glds16(VT + ch * (NV * 1024) + (14 + wv) * 1024, lane * 16, sl + AT_V + (14 + wv) * 1024);
  };
  const rsrc_t wsr = mk_rsrc(ws, WS_END);
  const int q0 = qb * 256 + qg * 64 + r32;
  const frag qaA = bld16(wsr, ((head * 2 + hi) * S + q0) * 16, WS_QA), qaB = bld16(wsr, ((head * 2 + hi) * S + q0) * 16, WS_QA + 512);
  u32x2 qbA = bld8(wsr, (head * S + q0) * 8, WS_QB), qbB = bld8(wsr, (head * S + q0) * 8, WS_QB + 256);
  stage(0);
  const int pk = (r32 & 0x13) | ((r32 & 4) << 1) | ((r32 & 8) >> 1);
  const unsigned kz = lds0 + hi * (AT_C * 16) + pk * 16, bz = lds0 + AT_KB + pk * 8;
  const unsigned ka0 = hi * (AT_C * 16) + pk * 16 + kh * 4096, kb0 = AT_KB + pk * 8 + kh * 2048;
  const unsigned v0 = AT_V + (hi * NV + min(r32, NV - 1)) * 16 + kh * (32 * NV * 16);
  WAITV(0);
  if (hi) { qbA = u32x2{0u, 0u}; qbB = u32x2{0u, 0u}; }
  PIN(qbA); PIN(qbB);
  BAR();
  stage(1); stage(2);
  STAMP(1);
  f32x16 oA = {}, oB = {}, nmA, nmB, sA, sB;
  frag ka, va, vb, plo, phi = {0u, 0u, 0u, 0u}; u32x2 kb;
  {
    ka = lds8(kz); kb = lds4(bz);
    sA = mfma32(ka, qaA, f32x16{}); sB = mfma32(ka, qaB, f32x16{});
    sA = mfma32k8(kb, qbA, sA); sB = mfma32k8(kb, qbB, sB);
    ka = lds8(lds0 + ka0); kb = lds4(lds0 + kb0); vb = lds8(lds0 + v0); va = lds8(lds0 + v0);
    const float mA = max16(sA), mB = max16(sB);
#pragma unroll
    for (int r = 0; r < 16; ++r) { nmA[r] = -mA; nmB[r] = -mB; }
    PIN(nmA); PIN(nmB);
    sA = mfma32(ka, qaA, nmA); sA = mfma32k8(kb, qbA, sA);
  }
#define EX4(C, i, P, w) do { C[i] = __builtin_amdgcn_exp2f(C[i]); C[i + 1] = __builtin_amdgcn_exp2f(C[i + 1]); C[i + 2] = __builtin_amdgcn_exp2f(C[i + 2]); \
    C[i + 3] = __builtin_amdgcn_exp2f(C[i + 3]); P[w] = pk2(C[i], C[i + 1]); P[w + 1] = pk2(C[i + 2], C[i + 3]); PIN(P); SBAR(); } while (0)
  for (int ch = 0; ch < 4; ++ch) {
    const unsigned cur = lds0 + (ch % 3) * AT_SLOT, nxt = lds0 + ((ch + 1) % 3) * AT_SLOT;
    const unsigned kc = cur + ka0, kn = nxt + ka0, bc = cur + kb0, bn = nxt + kb0, vc = cur + v0, vn = nxt + v0;
#pragma unroll
    for (int t = 0; t < 8; ++t) {
      if (t == 6 && ch < 3) { WAITV(0); RAWBAR(); if (ch == 1) stage(3); }
      sB = mfma32(ka, qaB, nmB); SBAR(); ka = lds8(t < 7 ? kc + (t + 1) * 512 : kn); SBAR(); EX4(sA, 0, plo, 0);
      oB = mfma32(vb, phi, oB); SBAR(); vb = lds8(vc + (4 * t + 2) * (NV * 16)); SBAR(); EX4(sA, 4, plo, 2);
      sB = mfma32k8(kb, qbB, sB); SBAR(); kb = lds4(t < 7 ? bc + (t + 1) * 256 : bn); SBAR(); EX4(sA, 8, phi, 0);
      oA = mfma32(va, plo, oA); SBAR(); EX4(sA, 12, phi, 2);
      sA = mfma32(ka, qaA, nmA); SBAR(); EX4(sB, 0, plo, 0);
      oA = mfma32(vb, phi, oA); SBAR(); EX4(sB, 4, plo, 2);
      sA = mfma32k8(kb, qbA, sA); SBAR(); EX4(sB, 8, phi, 0);
      oB = mfma32(va, plo, oB); SBAR(); va = lds8(t < 7 ? vc + (4 * t + 4) * (NV * 16) : vn); SBAR(); EX4(sB, 12, phi, 2);
    }
  }
  oB = mfma32(vb, phi, oB);
#undef EX4
  STAMP(2);
  f32x16 o;
  {
    PIN(oA); PIN(oB);
    const unsigned rw = lds0 + AT_RED + wv * 3072 + lane * 16, rd = lds0 + AT_RED + (wv ^ 4) * 3072 + lane * 16;
#pragma unroll
    for (int i = 0; i < 3; ++i) {
      f32x4 w;
#pragma unroll
      for (int e = 0; e < 4; ++e) { w[e] = kh ? oA[4 * i + e] : oB[4 * i + e]; o[4 * i + e] = kh ? oB[4 * i + e] : oA[4 * i + e]; }
      *(LAS f32x4*)(rw + i * 1024) = w;
    }
    BAR();
#pragma unroll
    for (int i = 0; i < 3; ++i) {
      const f32x4 w = *(const LAS f32x4*)(rd + i * 1024);
#pragma unroll
      for (int e = 0; e < 4; ++e) o[4 * i + e] += w[e];
    }
  }
  float l = __shfl(o[8], r32 + 32);
  float chk = 0.f;
#pragma unroll
  for (int r = 0; r < 12; ++r) chk += __builtin_fabsf(o[r]);
  if (__builtin_expect(__any(!(chk < 3.0e38f)), 0)) {
    const frag qa = kh ? qaB : qaA; const u32x2 qb4 = kh ? qbB : qbA;
    o = f32x16{};
    float m = -INFINITY;
    for (int t = 0; t < 64; ++t) {
      const frag k1 = *(const frag*)(KA + (hi * S + t * 32 + pk) * 16);
      const u32x2 k2 = *(const u32x2*)(KB + (t * 32 + pk) * 8);
      const char* vp = VT + ((t * 4 + hi) * NV + min(r32, NV - 1)) * 16;
      const frag v1 = *(const frag*)vp, v2 = *(const frag*)(vp + 2 * NV * 16);
      f32x16 s = mfma32(k1, qa, f32x16{}); s = mfma32k8(k2, qb4, s);
      const float mn = __builtin_fmaxf(m, max16(s));
      const float al = __builtin_amdgcn_exp2f(m - mn);
      m = mn;
#pragma unroll
      for (int r = 0; r < 16; ++r) { s[r] = __builtin_amdgcn_exp2f(s[r] - mn); o[r] *= al; }
      o = mfma32(v1, pack8(s, 0), o); o = mfma32(v2, pack8(s, 8), o);
    }
    l = __shfl(o[8], r32 + 32);
  }
  const float rl = 1.0f / l;
  const int pos = pinv(32 * kh + r32);
  const int vo = (qb * 4 + qg) * (80 * 1024) + (pos >> 4) * 1024 + (pos & 15) * 16;
  bst16(wsr, vo + hi * 256, WS_O + (head >> 1) * 4096 + (head & 1) * 512,
        u32x4{pk2(o[0] * rl, o[1] * rl), pk2(o[2] * rl, o[3] * rl), pk2(o[4] * rl, o[5] * rl), pk2(o[6] * rl, o[7] * rl)});
  if (!hi) bst8(wsr, vo, WS_O + (16 + (head >> 3)) * 4096 + ((head >> 1) & 3) * 256 + (head & 1) * 8,
                u32x2{pk2(o[8] * rl, o[9] * rl), pk2(o[10] * rl, o[11] * rl)});
  STAMP_END(ws, 2, 3);
}

constexpr int P_X = 140 * 1024, P_LDS = P_X + 10240;
template <bool LAST>
__global__ __launch_bounds__(576) void k_proj(const u16* __restrict__ x, u16* __restrict__ out, char* __restrict__ ws) {
  extern __shared__ __attribute__((aligned(16))) char smem[];
  constexpr int NKB = LAST ? 14 : 10, NKS = 2 * NKB;
  const int tid = TID, lane = tid & 63, wv = ufl(tid >> 6);
  const int pi = (BID & 7) * 4 + ((BID >> 3) & 3), j = BID >> 5;
  const unsigned lds0 = (unsigned)(uintptr_t)smem;
  TS_DECL; STAMP(0);
  if (wv >= 4) {
    const int lw = wv - 4;
    const char* src = ws + (LAST ? WS_WOUT : WS_WO) + (j * NKS * 5 + lw) * 1024;
    const unsigned dst = lds0 + lw * 1024;
    constexpr int NX = LAST ? 0 : 2;
    auto issue = [&](int kb) {
      if (kb < NKB) { glds16(src + kb * 10240, lane * 16, dst + kb * 10240); glds16(src + kb * 10240 + 5120, lane * 16, dst + kb * 10240 + 5120); }
      if (kb == NKB && !LAST) {
#pragma unroll
        for (int i = 0; i < 2; ++i) {
          const int ch = (lw * 2 + i) * 64 + lane, row = ch / 10;
          glds16(x + (size_t)pi * 64 * D + 80 * j, prow(row >> 4, row & 15) * (D * 2) + (ch - row * 10) * 16, lds0 + P_X + (lw * 2 + i) * 1024);
        }
      }
    };
    auto upto = [](int kb) { return kb < 3 ? 2 + 2 * kb : kb + 5; };
    issue(0); issue(1);
#pragma unroll
    for (int kb = 0; kb < NKB; ++kb) {
      const int n = upto(kb);
      WAITV((n > NKB ? 2 * NKB + NX : 2 * n) - 2 * (kb + 1));
      RAWBAR();
#pragma unroll
      for (int i = n; i < upto(kb + 1); ++i) issue(i);
    }
    if (!LAST) {
      WAITV(0); RAWBAR();
      if (lw == 0 && BID < 224) {
        const rsrc_t wsr = mk_rsrc(ws, WS_END);
        float u = 0.f, v = 0.f;
#pragma unroll
        for (int i = 0; i < 5; ++i) {
          const u32x2 t = bld8(wsr, ((lane >> 3) * 5 * 1792 + BID * 8 + (lane & 7)) * 8, WS_UVP + i * 1792 * 8);
          u += __uint_as_float(t[0]); v += __uint_as_float(t[1]);
        }
        u = dpp_add<0x128>(u); v = dpp_add<0x128>(v);
        u += __shfl_xor(u, 16); v += __shfl_xor(v, 16); u += __shfl_xor(u, 32); v += __shfl_xor(v, 32);
        if (lane < 8) bst8(wsr, (BID * 8 + lane) * 8, WS_UV, u32x2{__float_as_uint(u), __float_as_uint(v)});
      }
    }
    STAMP_END(ws, (LAST ? 6 : 3), 9);
    return;
  }
  const rsrc_t ar = mk_rsrc(ws + (LAST ? WS_U : WS_O) + pi * (NKS * 4096) + wv * 1024, NKS * 4096);
  frag a[NKS], b[2][5];
  auto upto = [](int ks) { return ks < 6 ? 4 + 2 * ks : ks + 10; };
#pragma unroll
  for (int i = 0; i < upto(0); ++i) a[i] = bld16(ar, lane * 16, i * 4096);
  SBAR();
  const int r = lane & 15, g = lane >> 4;
  const rsrc_t wsr = mk_rsrc(ws, WS_END);
  const unsigned bb = lds0 + lane * 16;
  const int go = (LAST ? 5 : 2) * D * 4 + 80 * j * 4;
  const int xo = pi * 81920 + wv * 1024 + lane * 16;
  u32x4 gv[5], rv[2]; u32x2 rt;
  f32x4 acc[5] = {};
  STAMP(4);
  RAWBAR();
  STAMP(5);
#pragma unroll
  for (int t = 0; t < 5; ++t) b[0][t] = lds8(bb + t * 1024);
#pragma unroll
  for (int ks = 0; ks < NKS; ++ks) {
    if (ks + 1 < NKS) {
      if (ks & 1) RAWBAR();
#pragma unroll
      for (int t = 0; t < 5; ++t) b[(ks + 1) & 1][t] = lds8(bb + ((ks + 1) * 5 + t) * 1024);
    }
    SBAR();
#pragma unroll
    for (int t = 0; t < 5; ++t) acc[t] = mfma16(b[ks & 1][t], a[ks], acc[t]);
    SBAR();
#pragma unroll
    for (int i = upto(ks); i < upto(ks + 1); ++i) {
      if (i < NKS) a[i] = bld16(ar, lane * 16, i * 4096);
      if (i == NKS) {
#pragma unroll
        for (int t = 0; t < 4; ++t) gv[t] = bld16(wsr, g * 32, WS_MOD + go + (t >> 1) * 128 + (t & 1) * 16);
        gv[4] = bld16(wsr, g * 16, WS_MOD + go + 256);
      }
      if (i == NKS + 1 && LAST) {
        rv[0] = bld16(wsr, xo, WS_X1 + 2 * j * 4096); rv[1] = bld16(wsr, xo, WS_X1 + (2 * j + 1) * 4096);
        rt = bld8(wsr, xo, WS_X1 + (16 + (j >> 1)) * 4096 + (j & 1) * 8);
      }
    }
    SBAR();
  }
  STAMP(2);
  const unsigned xa = lds0 + P_X + (wv * 16 + r) * 160;
  if (!LAST) { RAWBAR(); rv[0] = lds8(xa + g * 16); rv[1] = lds8(xa + 64 + g * 16); rt = lds4(xa + 128 + g * 8); }
  u32x4 y[2];
#pragma unroll
  for (int p = 0; p < 2; ++p)
    y[p] = pk8(up4(rv[p][0], rv[p][1]) + __builtin_bit_cast(f32x4, gv[2 * p]) * acc[2 * p],
               up4(rv[p][2], rv[p][3]) + __builtin_bit_cast(f32x4, gv[2 * p + 1]) * acc[2 * p + 1]);
  const u32x2 yt = pk4(up4(rt[0], rt[1]) + __builtin_bit_cast(f32x4, gv[4]) * acc[4]);
  if (!LAST) {
    bst16(wsr, xo, WS_X1 + 2 * j * 4096, y[0]); bst16(wsr, xo, WS_X1 + (2 * j + 1) * 4096, y[1]);
    bst8(wsr, xo, WS_X1 + (16 + (j >> 1)) * 4096 + (j & 1) * 8, yt);
    float s1 = 0.f, s2 = 0.f;
#pragma unroll
    for (int e = 0; e < 10; ++e) { const unsigned w = e < 8 ? y[e >> 2][e & 3] : yt[e & 1]; s1 = dot2(w, 0x3f803f80u, s1); s2 = dot2(w, w, s2); }
    s1 += __shfl_xor(s1, 16); s2 += __shfl_xor(s2, 16); s1 += __shfl_xor(s1, 32); s2 += __shfl_xor(s2, 32);
    if (g == 0) bst8(wsr, ((pi * 8 + j) * 64 + wv * 16 + r) * 8, WS_XST, u32x2{__float_as_uint(s1), __float_as_uint(s2)});
  } else {
    *(LAS u32x4*)(xa + g * 16) = y[0]; *(LAS u32x4*)(xa + 64 + g * 16) = y[1]; *(LAS u32x2*)(xa + 128 + g * 8) = yt;
    const rsrc_t orr = mk_rsrc(out + (size_t)pi * 64 * D + 80 * j, 64 * D * 2 - 160 * j);
#pragma unroll
    for (int i = 0; i < 3; ++i) {
      const int ch = i * 64 + lane, row = ch / 10;
      if (ch < 160) bst16(orr, prow(wv, row) * (D * 2) + (ch - row * 10) * 16, 0, lds8(lds0 + P_X + wv * 2560 + ch * 16));
    }
  }
  STAMP_END(ws, (LAST ? 6 : 3), 3);
}

constexpr int W_PART = 81920, W_LN = W_PART + 4096, W_LDS = W_LN + 528;
__global__ __launch_bounds__(512, 2) void k_win(char* __restrict__ ws) {
  extern __shared__ __attribute__((aligned(16))) char smem[];
  const int tid = TID, lane = tid & 63, wv = (ufl(tid >> 6) + 7) & 7;
  const int pi = (BID & 7) * 4 + ((BID >> 3) & 3), j = BID >> 5;
  const unsigned lds0 = (unsigned)(uintptr_t)smem;
  LAS f32x2* part = (LAS f32x2*)(lds0 + W_PART);
  LAS f32x2* lnp = (LAS f32x2*)(lds0 + W_LN);
  LAS unsigned* cnt = (LAS unsigned*)(lds0 + W_LN + 512);
  TS_DECL; STAMP(0);
  const int r = lane & 15, g = lane >> 4;
  const char* src = ws + WS_X1 + pi * 81920;
  const unsigned ab = lds0 + lane * 16;
  const rsrc_t wsr = mk_rsrc(ws, WS_END);
  GAS unsigned long long* gran = (GAS unsigned long long*)(ws + WS_STAT) + pi * 512;
  if (wv == 7) {
    if (lane == 0) *cnt = 0u;
    u32x2 xs[8];
#pragma unroll
    for (int w = 0; w < 8; ++w) xs[w] = bld8(wsr, ((pi * 8 + w) * 64 + lane) * 8, WS_XST);
    auto issue = [&](int kb) {
      if (kb < 10) {
#pragma unroll
        for (int i = 0; i < 8; ++i) glds16(src + kb * 8192 + i * 1024, lane * 16, lds0 + kb * 8192 + i * 1024);
      }
    };
    issue(0); issue(1);
#pragma unroll
    for (int kb = 0; kb < 10; ++kb) {
      issue(kb + 2);
      WAITV(kb < 8 ? 16 : (kb < 9 ? 8 : 0));
      RAWBAR();
    }
    {
      float s1 = 0.f, s2 = 0.f;
#pragma unroll
      for (int w = 0; w < 8; ++w) { s1 += __uint_as_float(xs[w][0]); s2 += __uint_as_float(xs[w][1]); }
      const float mu = s1 * (1.0f / D);
      const float rs = __builtin_amdgcn_rsqf(__builtin_fmaxf(s2 * (1.0f / D) - mu * mu, 0.f) + 1e-5f);
      lnp[lane] = f32x2{rs, -mu * rs};
    }
    BAR();
    for (unsigned spins = 0; __hip_atomic_load(cnt, __ATOMIC_RELAXED, __HIP_MEMORY_SCOPE_WORKGROUP) < 7u && spins < (1u << 24); ++spins) __builtin_amdgcn_s_sleep(1);
    asm volatile("" ::: "memory");
    float t1 = 0.f, t2 = 0.f;
#pragma unroll
    for (int w = 0; w < 7; ++w) { f32x2 p = part[w * 64 + lane]; t1 += p[0]; t2 += p[1]; }
    unsigned b1w = __float_as_uint(t1), b2w = __float_as_uint(t2);
    if (b2w == ~0u) b2w = 0x7fc00000u;
    __hip_atomic_store(gran + j * 64 + lane, ((unsigned long long)b2w << 32) | b1w, __ATOMIC_RELAXED, __HIP_MEMORY_SCOPE_AGENT);
    for (unsigned spins = 0;; ++spins) {
      bool ok = true; t1 = 0.f; t2 = 0.f;
#pragma unroll
      for (int w = 0; w < 8; ++w) {
        unsigned long long v = __hip_atomic_load(gran + w * 64 + lane, __ATOMIC_RELAXED, __HIP_MEMORY_SCOPE_AGENT);
        ok &= (unsigned)(v >> 32) != ~0u;
        t1 += __uint_as_float((unsigned)v); t2 += __uint_as_float((unsigned)(v >> 32));
      }
      if (__all(ok) || spins > (1u << 20)) break;
      __builtin_amdgcn_s_sleep(1);
    }
    const float mu = t1 * (1.0f / FH);
    const float rs = __builtin_amdgcn_rsqf(__builtin_fmaxf(t2 * (1.0f / FH) - mu * mu, 0.f) + 1e-5f);
    part[7 * 64 + lane] = f32x2{rs, -mu * rs};
    STAMP(3);
    BAR();
    STAMP_END(ws, 4, 9);
    return;
  }
  const rsrc_t wr = mk_rsrc(ws + WS_WIN + (j * 14 + wv) * 20 * 1024, 8 * 20 * 1024);
  frag bw[40];
  auto ldb = [&](int i) { if (i < 40) bw[i] = bld16(wr, lane * 16, (i < 20 ? i : 120 + i) * 1024); };
  constexpr int AH = 4;
#pragma unroll
  for (int i = 0; i < AH; ++i) { ldb(2 * i); ldb(2 * i + 1); }
  const u32x2 uv2 = bld8(wsr, ((j * 14 + wv) * 16 + r) * 8, WS_UV);
  const u32x4 uv1a = bld16(wsr, ((j * 14 + 7 + wv) * 16 + 4 * g) * 8, WS_UV), uv1b = bld16(wsr, ((j * 14 + 7 + wv) * 16 + 4 * g) * 8, WS_UV + 16);
  f32x4 z2[4] = {}, z1[4] = {};
  frag a[2][8];
  RAWBAR();
  STAMP(1);
#pragma unroll
  for (int i = 0; i < 8; ++i) a[0][i] = lds8(ab + i * 1024);
#pragma unroll
  for (int kb = 0; kb < 20; ++kb) {
    if (kb < 9) RAWBAR();
    if (kb + 1 < 20) {
#pragma unroll
      for (int i = 0; i < 8; ++i) a[(kb + 1) & 1][i] = lds8(ab + ((kb + 1) % 10) * 8192 + i * 1024);
    }
    SBAR();
#pragma unroll
    for (int i = 0; i < 8; ++i) {
      if (kb < 10) z2[i & 3] = mfma16(a[kb & 1][i], bw[2 * kb + (i >> 2)], z2[i & 3]);
      else z1[i & 3] = mfma16(bw[2 * kb + (i >> 2)], a[kb & 1][i], z1[i & 3]);
    }
    SBAR();
    if (kb < 10) { ldb(2 * (kb + AH)); ldb(2 * (kb + AH) + 1); }
    SBAR();
    if (kb == 9) {
      STAMP(6);
      RAWBAR();
      const float u = __uint_as_float(uv2[0]), v = __uint_as_float(uv2[1]);
      f32x4 ln[4][2];
#pragma unroll
      for (int mt = 0; mt < 4; ++mt) { ln[mt][0] = *(const LAS f32x4*)&lnp[mt * 16 + 4 * g]; ln[mt][1] = *(const LAS f32x4*)&lnp[mt * 16 + 4 * g + 2]; }
      f32x2 st[4];
#pragma unroll
      for (int mt = 0; mt < 4; ++mt) {
        f32x4 z, q;
#pragma unroll
        for (int e = 0; e < 4; ++e)
          z[e] = gelu_tanh(__builtin_fmaf(z2[mt][e], ln[mt][e >> 1][2 * (e & 1)], __builtin_fmaf(ln[mt][e >> 1][2 * (e & 1) + 1], u, v)));
        const u32x2 zb = pk4(z);
        z = up4(zb[0], zb[1]); q = z * z;
        z2[mt] = z;
        st[mt] = f32x2{quad4_sum(z), quad4_sum(q)};
        PIN(st[mt]);
        SBAR(); ldb(28 + 3 * mt); ldb(29 + 3 * mt); ldb(30 + 3 * mt); SBAR();
      }
      if ((r & 3) == 0) {
#pragma unroll
        for (int mt = 0; mt < 4; ++mt) part[wv * 64 + mt * 16 + 4 * g + (r >> 2)] = st[mt];
      }
      asm volatile("s_waitcnt lgkmcnt(0)" ::: "memory");
      if (lane == 0) __hip_atomic_fetch_add(cnt, 1u, __ATOMIC_RELAXED, __HIP_MEMORY_SCOPE_WORKGROUP);
      STAMP(2);
    }
  }
  STAMP(7);
  {
    const f32x4 ua = __builtin_bit_cast(f32x4, uv1a), ub = __builtin_bit_cast(f32x4, uv1b);
#pragma unroll
    for (int mt = 0; mt < 4; ++mt) {
      const f32x2 ln = lnp[mt * 16 + r];
      z1[mt][0] = gelu_tanh(__builtin_fmaf(z1[mt][0], ln[0], __builtin_fmaf(ln[1], ua[0], ua[1])));
      z1[mt][1] = gelu_tanh(__builtin_fmaf(z1[mt][1], ln[0], __builtin_fmaf(ln[1], ua[2], ua[3])));
      z1[mt][2] = gelu_tanh(__builtin_fmaf(z1[mt][2], ln[0], __builtin_fmaf(ln[1], ub[0], ub[1])));
      z1[mt][3] = gelu_tanh(__builtin_fmaf(z1[mt][3], ln[0], __builtin_fmaf(ln[1], ub[2], ub[3])));
    }
    const int zo = ((pi * 8 + j) * 7 + wv) * 2048 + lane * 16;
    bst16(wsr, zo, WS_Z1, pk8(z1[0], z1[1])); bst16(wsr, zo, WS_Z1 + 1024, pk8(z1[2], z1[3]));
  }
  BAR();
  STAMP(4);
#pragma unroll
  for (int mt = 0; mt < 4; ++mt)
#pragma unroll
    for (int e = 0; e < 4; e += 2) {
      const f32x4 p = *(const LAS f32x4*)&part[7 * 64 + mt * 16 + 4 * g + e];
      z2[mt][e] = __builtin_fmaf(z2[mt][e], p[0], p[1]); z2[mt][e + 1] = __builtin_fmaf(z2[mt][e + 1], p[2], p[3]);
    }
  const int zo = ((j * 7 + wv) * 64 + pi * 2) * 1024 + lane * 16;
  bst16(wsr, zo, WS_ZT, pk8(z2[0], z2[1])); bst16(wsr, zo, WS_ZT + 1024, pk8(z2[2], z2[3]));
  STAMP_END(ws, 4, 5);
}

constexpr int G_W = 4, G_RING = 8, G_RED = G_RING * 8192, G_LDS = G_RED + 8 * 4096;
template <int NT, bool FIRST, class F>
DEVI void sp_loop(f32x4 (&acc)[2][4], rsrc_t br, int voff, unsigned ab, int rot, F&& late TS_ARGS) {
  frag bw[G_W][2], a[2][4];
  auto issue = [&](int kb, int i) {
    const int o = kb < 32 ? ((kb + rot) & 31) * 2048 : (1 << 20);
    bw[i][0] = bld16(br, voff, o);
    if (NT > 1) bw[i][1] = bld16(br, voff + 65536, o);
  };
#pragma unroll
  for (int i = 0; i < G_W - (FIRST ? 1 : 0); ++i) issue(i, i);
  late();
  STAMP(5);
  RAWBAR();
  STAMP(7);
#pragma unroll
  for (int mt = 0; mt < 4; ++mt) a[0][mt] = lds8(ab + mt * 2048);
#pragma nounroll
  for (int k0 = 0; k0 < 32; k0 += G_RING) {
#pragma unroll
    for (int sl = 0; sl < G_RING; ++sl) {
      RAWBAR();
#pragma unroll
      for (int mt = 0; mt < 4; ++mt) a[(sl + 1) & 1][mt] = lds8(ab + ((sl + 1) % G_RING) * 8192 + mt * 2048);
      SBAR();
      if (FIRST) { issue(k0 + sl + G_W - 1, (sl + G_W - 1) % G_W); SBAR(); }
#pragma unroll
      for (int t = 0; t < NT; ++t)
#pragma unroll
        for (int mt = 0; mt < 4; ++mt) acc[t][mt] = mfma16(bw[sl % G_W][t], a[sl & 1][mt], acc[t][mt]);
      SBAR();
      if (!FIRST) { issue(k0 + sl + G_W, sl % G_W); SBAR(); }
    }
  }
  STAMP(10);
}
__global__ __launch_bounds__(640) void k_sp(const u16* __restrict__ w_sp, const u16* __restrict__ b_sp, char* __restrict__ ws) {
  extern __shared__ __attribute__((aligned(16))) char smem[];
  const int tid = TID, lane = tid & 63, hw = ufl(tid >> 6), wv = hw - 2;
  const int xg = BID & 7, bi = BID >> 3;
  const int pi = (xg >> 1) * 8 + (bi & 7), j = (xg & 1) * 4 + (bi >> 3);
  const unsigned lds0 = (unsigned)(uintptr_t)smem;
#define ROT bi
  TS_DECL; STAMP(0);
  if (hw < 2) {
    const int rr = lane >> 3;
    const char* src = (const char*)w_sp + (size_t)(pi * 64 + hw * 32) * (S * 2);
    const unsigned sv0 = prow(0, rr) * (S * 2) + (((lane & 7) ^ rr) << 4), dst = lds0 + hw * 4096;
    auto issue = [&](int kb) {
      const char* s = src + ((kb + ROT) & 31) * 128;
      const unsigned d = dst + (kb & (G_RING - 1)) * 8192;
      glds16(s, sv0, d); glds16(s + 16 * (S * 2), sv0, d + 1024); glds16(s + 4 * (S * 2), sv0, d + 2048); glds16(s + 20 * (S * 2), sv0, d + 3072);
    };
    constexpr int AH = 4;
    for (int kb = 0; kb < AH; ++kb) issue(kb);
#pragma nounroll
    for (int kb = 0; kb < 32 - AH; ++kb) { WAITV(4 * (AH - 1)); RAWBAR(); issue(kb + AH); }
    WAITV(12); RAWBAR(); WAITV(8); RAWBAR(); WAITV(4); RAWBAR(); WAITV(0); RAWBAR();
    STAMP_END(ws, 5, 9);
    return;
  }
  const int r = lane & 15, p = wv >> 1, kh = wv & 1;
  const rsrc_t br = mk_rsrc(ws + WS_ZT + (j * 7 + 2 * p) * 65536, 2 * 65536);
  const rsrc_t wsr = mk_rsrc(ws, WS_END);
  const int voff = lane * 16 + kh * 1024;
  const unsigned ab = afrag_base(lds0, lane, kh);
  u32x4 z1v[2]; unsigned bsv[2];
  auto late = [&]() {
#pragma unroll
    for (int i = 0; i < 2; ++i) {
      bsv[i] = b_sp[pi * 64 + prow(2 * kh + i, r)];
      z1v[i] = bld16(wsr, ((pi * 8 + j) * 7 + min(2 * p + i, 6)) * 2048 + kh * 1024 + lane * 16, WS_Z1);
    }
  };
  f32x4 acc[2][4] = {};
  if (p < 2) sp_loop<2, true>(acc, br, voff, ab, ROT, late TS_PASS);
  else if (p == 2) sp_loop<2, false>(acc, br, voff, ab, ROT, late TS_PASS);
  else sp_loop<1, false>(acc, br, voff, ab, ROT, late TS_PASS);
  const unsigned rw = lds0 + G_RED + wv * 4096 + lane * 16, rd = lds0 + G_RED + (wv ^ 1) * 4096 + lane * 16;
  f32x4 c[2][2];
#pragma unroll
  for (int t = 0; t < 2; ++t)
#pragma unroll
    for (int i = 0; i < 2; ++i) {
      f32x4 lo = acc[t][i], hi = acc[t][2 + i];
      PIN(lo); PIN(hi);
      *(LAS f32x4*)(rw + (t * 2 + i) * 1024) = kh ? lo : hi;
      c[t][i] = kh ? hi : lo;
    }
  BAR();
#pragma unroll
  for (int t = 0; t < 2; ++t)
#pragma unroll
    for (int i = 0; i < 2; ++i) c[t][i] += *(const LAS f32x4*)(rd + (t * 2 + i) * 1024);
  STAMP(2);
#pragma unroll
  for (int i = 0; i < 2; ++i) {
    const float bs = bf_lo(bsv[i]);
    const f32x4 y0 = up4(z1v[0][2 * i], z1v[0][2 * i + 1]) * (c[0][i] + bs), y1 = up4(z1v[1][2 * i], z1v[1][2 * i + 1]) * (c[1][i] + bs);
    if (p < 3) bst16(wsr, (pi * 28 + 3 * j + p) * 4096 + kh * 2048 + lane * 16, WS_U + i * 1024, pk8(y0, y1));
    else bst8(wsr, (pi * 28 + 24 + (j >> 1)) * 4096 + kh * 2048 + lane * 16 + (j & 1) * 8, WS_U + i * 1024, pk4(y0));
  }
  STAMP_END(ws, 5, 3);
}

#if defined(__HIP_DEVICE_COMPILE__)
#pragma clang attribute pop
#endif
extern "C" void kernel_launch(void* const* d_in, const int* in_sizes, int n_in, void* d_out, int out_size, void* d_ws,
                              size_t ws_size, hipStream_t stream) {
  const u16* x = (const u16*)d_in[0];
  const u16* wqkv = (const u16*)d_in[1];
  const u16* wo = (const u16*)d_in[2];
  const u16* w_in = (const u16*)d_in[4];
  const u16* w_sp = (const u16*)d_in[5];
  const u16* b_sp = (const u16*)d_in[6];
  const u16* w_out = (const u16*)d_in[7];
  const u16* c = (const u16*)d_in[8];
  const u16* w_mod = (const u16*)d_in[9];
  char* ws = (char*)d_ws;
  (void)hipFuncSetAttribute((const void*)k_qkv, hipFuncAttributeMaxDynamicSharedMemorySize, Q_LDS);
  (void)hipFuncSetAttribute((const void*)k_attn, hipFuncAttributeMaxDynamicSharedMemorySize, AT_LDS);
  (void)hipFuncSetAttribute((const void*)k_proj<false>, hipFuncAttributeMaxDynamicSharedMemorySize, P_LDS);
  (void)hipFuncSetAttribute((const void*)k_proj<true>, hipFuncAttributeMaxDynamicSharedMemorySize, P_LDS);
  (void)hipFuncSetAttribute((const void*)k_win, hipFuncAttributeMaxDynamicSharedMemorySize, W_LDS);
  (void)hipFuncSetAttribute((const void*)k_sp, hipFuncAttributeMaxDynamicSharedMemorySize, G_LDS);
#define REP
  REP k_prep<<<256 + PT_BLOCKS, 256, 0, stream>>>(c, w_mod, wqkv, wo, w_out, ws);
  REP k_qkv<<<256, 768, Q_LDS, stream>>>(x, ws);
  REP k_attn<<<256, 768, AT_LDS, stream>>>(w_in, ws);
  REP k_proj<false><<<256, 576, P_LDS, stream>>>(x, (u16*)d_out, ws);
  REP k_win<<<256, 512, W_LDS, stream>>>(ws);
  REP k_sp<<<256, 640, G_LDS, stream>>>(w_sp, b_sp, ws);
  REP k_proj<true><<<256, 576, P_LDS, stream>>>(x, (u16*)d_out, ws);
}
```

```cpp
#if defined(__HIP_DEVICE_COMPILE__)
#pragma clang attribute push(__attribute__((target("no-packed-fp32-ops"))), apply_to = function)
#endif
#include <hip/hip_runtime.h>
#include <hip/hip_bf16.h>
#include <stdint.h>

typedef unsigned short u16;
typedef __attribute__((ext_vector_type(8))) __bf16 bf16v8;
typedef __attribute__((ext_vector_type(2))) __bf16 bf16x2;
typedef __attribute__((ext_vector_type(4))) short s16x4;
typedef __attribute__((ext_vector_type(2))) float f32x2;
typedef __attribute__((ext_vector_type(4))) float f32x4;
typedef __attribute__((ext_vector_type(16))) float f32x16;
typedef __attribute__((ext_vector_type(4))) unsigned u32x4;
typedef __attribute__((ext_vector_type(2))) unsigned u32x2;
typedef u32x4 frag;
#define LAS __attribute__((address_space(3)))
#define GAS __attribute__((address_space(1)))
#define DEVI __device__ __forceinline__
#define TID ((int)__builtin_amdgcn_workitem_id_x())
#define BID ((int)__builtin_amdgcn_workgroup_id_x())

constexpr int S = 2048, D = 640, NH = 32, DH = 20, FF = 1728, FH = 864;
constexpr int NV = 22;

constexpr int WS_MOD  = 0;
constexpr int WS_UV   = 16384;
constexpr int WS_STAT = 32768;
constexpr int WS_CTR  = WS_STAT + 32 * 8 * 64 * 8;
constexpr int WS_XST  = WS_CTR + 128 * 128;
constexpr int WS_UVP  = WS_XST + 32 * 8 * 64 * 8;
constexpr int WS_WQKV = WS_UVP + 40 * 1792 * 8;
constexpr int WS_WO   = WS_WQKV + 8 * 15 * 20 * 1024;
constexpr int WS_WIN  = WS_WO + 40 * 20 * 1024;
constexpr int WS_WOUT = WS_WIN + 8 * 14 * 20 * 1024;
constexpr int WS_QA   = WS_WOUT + 40 * 28 * 1024;
constexpr int WS_QB   = WS_QA + NH * S * 32;
constexpr int WS_KA   = WS_QB + NH * S * 8;
constexpr int WS_KB   = WS_KA + NH * S * 32;
constexpr int WS_VT   = WS_KB + NH * S * 8;
constexpr int WS_O    = WS_VT + NH * 256 * NV * 16;
constexpr int WS_X1   = WS_O + S * D * 2;
constexpr int WS_ZT   = WS_X1 + S * D * 2;
constexpr int WS_Z1   = WS_ZT + 8 * 7 * 64 * 1024;
constexpr int WS_U    = WS_Z1 + 32 * 8 * 7 * 2 * 1024;
constexpr int WS_END  = WS_U + 32 * 28 * 4 * 1024;

#define TS_DECL
#define TS_ARGS
#define TS_PASS
#define STAMP(s)
#define STAMP_END(ws, k, s)

DEVI float bf_lo(unsigned w) { return __uint_as_float(w << 16); }
DEVI float bf_hi(unsigned w) { return __uint_as_float(w & 0xffff0000u); }
DEVI float bf2f(u16 h) { return __uint_as_float((unsigned)h << 16); }
DEVI unsigned pk2(float a, float b) { bf16x2 v = {(__bf16)a, (__bf16)b}; return __builtin_bit_cast(unsigned, v); }
DEVI float rbf(float a) { return bf_lo(pk2(a, a)); }
DEVI u32x2 pk4(const f32x4& a) { return u32x2{pk2(a[0], a[1]), pk2(a[2], a[3])}; }
DEVI u32x4 pk8(const f32x4& a, const f32x4& b) { return u32x4{pk2(a[0], a[1]), pk2(a[2], a[3]), pk2(b[0], b[1]), pk2(b[2], b[3])}; }
DEVI u32x4 pkI(const f32x4& a, const f32x4& b) { return u32x4{pk2(a[0], b[0]), pk2(a[1], b[1]), pk2(a[2], b[2]), pk2(a[3], b[3])}; }
DEVI f32x4 upE(const u32x4& w) { return f32x4{bf_lo(w[0]), bf_lo(w[1]), bf_lo(w[2]), bf_lo(w[3])}; }
DEVI f32x4 upO(const u32x4& w) { return f32x4{bf_hi(w[0]), bf_hi(w[1]), bf_hi(w[2]), bf_hi(w[3])}; }
DEVI f32x4 up4(unsigned a, unsigned b) { return f32x4{bf_lo(a), bf_hi(a), bf_lo(b), bf_hi(b)}; }
DEVI float dot2(unsigned a, unsigned b, float c) { return __builtin_amdgcn_fdot2_f32_bf16(__builtin_bit_cast(bf16x2, a), __builtin_bit_cast(bf16x2, b), c, false); }
DEVI f32x4 mfma16(frag a, frag b, f32x4 c) { return __builtin_amdgcn_mfma_f32_16x16x32_bf16(__builtin_bit_cast(bf16v8, a), __builtin_bit_cast(bf16v8, b), c, 0, 0, 0); }
DEVI f32x16 mfma32(frag a, frag b, f32x16 c) { return __builtin_amdgcn_mfma_f32_32x32x16_bf16(__builtin_bit_cast(bf16v8, a), __builtin_bit_cast(bf16v8, b), c, 0, 0, 0); }
DEVI f32x16 mfma32k8(u32x2 a, u32x2 b, f32x16 c) { return __builtin_amdgcn_mfma_f32_32x32x8bf16_1k(__builtin_bit_cast(s16x4, a), __builtin_bit_cast(s16x4, b), c, 0, 0, 0); }
DEVI frag lds8(unsigned a) { return *(const LAS frag*)a; }
DEVI u32x2 lds4(unsigned a) { return *(const LAS u32x2*)a; }
typedef __amdgpu_buffer_rsrc_t rsrc_t;
DEVI rsrc_t mk_rsrc(const void* p, int bytes) { return __builtin_amdgcn_make_buffer_rsrc((void*)p, 0, bytes, 0x00020000); }
DEVI frag bld16(rsrc_t r, int voff, int soff) { return __builtin_amdgcn_raw_buffer_load_b128(r, voff, soff, 0); }
DEVI u32x2 bld8(rsrc_t r, int voff, int soff) { return __builtin_amdgcn_raw_buffer_load_b64(r, voff, soff, 0); }
#define ST_AUX 0
DEVI void bst16(rsrc_t r, int voff, int soff, u32x4 v) { __builtin_amdgcn_raw_buffer_store_b128(v, r, voff + soff, 0, ST_AUX); }
DEVI void bst8(rsrc_t r, int voff, int soff, u32x2 v) { __builtin_amdgcn_raw_buffer_store_b64(v, r, voff, soff, ST_AUX); }
DEVI void bst16c(rsrc_t r, int voff, int soff, u32x4 v) { __builtin_amdgcn_raw_buffer_store_b128(v, r, voff + soff, 0, 16); }
DEVI void bst8c(rsrc_t r, int voff, int soff, u32x2 v) { __builtin_amdgcn_raw_buffer_store_b64(v, r, voff, soff, 16); }
DEVI u32x2 bld8c(rsrc_t r, int voff, int soff) { return __builtin_amdgcn_raw_buffer_load_b64(r, voff, soff, 16); }
DEVI frag bld16c(rsrc_t r, int voff, int soff) { return __builtin_amdgcn_raw_buffer_load_b128(r, voff, soff, 16); }
DEVI void glds16(const void* base, unsigned voff, unsigned lds_base) {
  unsigned sv;
  asm volatile("s_mov_b32 %0, m0\n\ts_mov_b32 m0, %3\n\ts_nop 0\n\tglobal_load_lds_dwordx4 %1, %2\n\ts_mov_b32 m0, %0"
               : "=&s"(sv) : "v"(voff), "s"(base), "s"(lds_base) : "memory");
}
DEVI void warm(const void* p, int n8k, int lane, unsigned scratch) {
  for (int i = 0; i < n8k; ++i) {
    unsigned sv;
    asm volatile("s_mov_b32 %0, m0\n\ts_mov_b32 m0, %3\n\ts_nop 0\n\tglobal_load_lds_dword %1, %2\n\ts_mov_b32 m0, %0"
                 : "=&s"(sv) : "v"(lane * 128 + i * 8192), "s"(p), "s"(scratch) : "memory");
  }
}
#define WAITV(n) asm volatile("s_waitcnt vmcnt(%0)" ::"i"(n) : "memory")
#define BAR() asm volatile("s_waitcnt lgkmcnt(0)\n\ts_barrier" ::: "memory")
#define RAWBAR() asm volatile("s_barrier" ::: "memory")
#define SBAR() __builtin_amdgcn_sched_barrier(0)
#define PIN(x) asm volatile("" : "+v"(x))
DEVI int ufl(int x) { return __builtin_amdgcn_readfirstlane(x); }
DEVI void signal_done(LAS unsigned* lcnt, unsigned n, GAS unsigned* gcnt, int lane) {
  WAITV(0);
  if (lane == 0 && __hip_atomic_fetch_add(lcnt, 1u, __ATOMIC_RELAXED, __HIP_MEMORY_SCOPE_WORKGROUP) == n - 1)
    __hip_atomic_fetch_add(gcnt, 1u, __ATOMIC_RELAXED, __HIP_MEMORY_SCOPE_AGENT);
}
template <int NAP> DEVI void wait_lds(LAS unsigned* lcnt, unsigned n) {
  for (unsigned spins = 0; (unsigned)ufl(__hip_atomic_load(lcnt, __ATOMIC_RELAXED, __HIP_MEMORY_SCOPE_WORKGROUP)) < n && spins < (1u << 22); ++spins) __builtin_amdgcn_s_sleep(NAP);
  asm volatile("" ::: "memory");
}
DEVI void wait_global(GAS unsigned* gcnt, unsigned n) {
  for (unsigned spins = 0; __hip_atomic_load(gcnt, __ATOMIC_RELAXED, __HIP_MEMORY_SCOPE_AGENT) < n && spins < (1u << 21); ++spins) __builtin_amdgcn_s_sleep(4);
  asm volatile("" ::: "memory");
}
template <int CTRL> DEVI float dpp_add(float v) {
  return v + __builtin_bit_cast(float, __builtin_amdgcn_update_dpp(0, __builtin_bit_cast(int, v), CTRL, 0xf, 0xf, true));
}
DEVI float row16_sum(float v) {
  v = dpp_add<0x128>(v); v = dpp_add<0x124>(v); v = dpp_add<0x122>(v); v = dpp_add<0x121>(v);
  return v;
}
DEVI float quad4_sum(const f32x4& v) {
  float x, y, o;
  asm("s_nop 1\n\t"
      "v_add_f32_dpp %0, %3, %3 row_ror:8 row_mask:0xf bank_mask:0x3\n\t"
      "v_add_f32_dpp %1, %4, %4 row_ror:8 row_mask:0xf bank_mask:0x3\n\t"
      "v_add_f32_dpp %0, %5, %5 row_ror:8 row_mask:0xf bank_mask:0xc\n\t"
      "v_add_f32_dpp %1, %6, %6 row_ror:8 row_mask:0xf bank_mask:0xc\n\t"
      "s_nop 0\n\t"
      "v_add_f32_dpp %2, %0, %0 row_half_mirror row_mask:0xf bank_mask:0x5\n\t"
      "v_add_f32_dpp %2, %1, %1 row_half_mirror row_mask:0xf bank_mask:0xa\n\t"
      "s_nop 1\n\t"
      "v_add_f32_dpp %2, %2, %2 quad_perm:[1,0,3,2] row_mask:0xf bank_mask:0xf\n\t"
      "s_nop 1\n\t"
      "v_add_f32_dpp %2, %2, %2 quad_perm:[2,3,0,1] row_mask:0xf bank_mask:0xf"
      : "=&v"(x), "=&v"(y), "=&v"(o) : "v"(v[0]), "v"(v[1]), "v"(v[2]), "v"(v[3]));
  return o;
}
DEVI float gelu_tanh(float x) {
  const float c1 = -2.0f * 0.7978845608028654f * 1.4426950408889634f, c3 = c1 * 0.044715f;
  float e = __builtin_amdgcn_exp2f(x * __builtin_fmaf(x * x, c3, c1));
  return x * __builtin_amdgcn_rcpf(1.0f + e);
}
DEVI u32x4 gated(const u32x4& res, const u32x4& g0, const u32x4& g1, const f32x4& ce, const f32x4& co) {
  const f32x4 a = __builtin_bit_cast(f32x4, g0), b = __builtin_bit_cast(f32x4, g1);
  return pkI(upE(res) + f32x4{a[0], a[2], b[0], b[2]} * ce, upO(res) + f32x4{a[1], a[3], b[1], b[3]} * co);
}
DEVI int prow(int mt, int r) { return 32 * (mt >> 1) + 8 * (r >> 2) + 4 * (mt & 1) + (r & 3); }
DEVI int pinv(int o) { return (o & ~31) | ((o & 4) << 2) | ((o >> 1) & 12) | (o & 3); }

DEVI void rows8(unsigned (&r)[8], rsrc_t w, int voff, int pitch, int k0, int k1) {
#pragma unroll
  for (int i = 0; i < 8; ++i) r[i] = __builtin_amdgcn_raw_buffer_load_b32(w, voff, ((i < 4 ? k0 : k1 - 4) + i) * pitch, 2);
}
template <bool WT> DEVI void put2(char* ws, int d0, int d1, const unsigned (&r)[8]) {
  u32x4 c0, c1;
#pragma unroll
  for (int i = 0; i < 4; ++i) {
    c0[i] = __builtin_amdgcn_perm(r[2 * i + 1], r[2 * i], 0x05040100u);
    c1[i] = __builtin_amdgcn_perm(r[2 * i + 1], r[2 * i], 0x07060302u);
  }
  const rsrc_t wsr = mk_rsrc(ws, WS_END);
  if (WT) { bst16c(wsr, d0, 0, c0); bst16c(wsr, d1, 0, c1); } else { bst16(wsr, d0, 0, c0); bst16(wsr, d1, 0, c1); }
}
DEVI int ncol(int o, int npair) { return o < 32 * npair ? (o & ~31) | ((o & 1) << 4) | ((o >> 1) & 15) : o; }
template <int W, int NP> DEVI void krows(int ks, int g, int& k0, int& k1, bool& ok) {
  ok = true;
  if (ks < 8 * NP) { const int j = ks / NP; k0 = W * j + 32 * (ks - j * NP) + 8 * g; k1 = k0 + 4; }
  else { k0 = W * 2 * (ks - 8 * NP) + 32 * NP + 4 * g; k1 = k0 + W; ok = 32 * NP + 4 * g < W; }
}
DEVI void tile_proj(unsigned (&r)[8], int& d0, int& d1, const u16* __restrict__ wo, const u16* __restrict__ w_out, int f, int lane) {
  const bool o = f < 400;
  if (!o) f -= 400;
  const int k8 = f / 5, n = (f - k8 * 5) * 128 + 2 * lane;
  int k0, k1; bool ok = true;
  if (!o) krows<108, 3>(k8 >> 2, k8 & 3, k0, k1, ok);
  else if (k8 < 64) { k0 = DH * (k8 >> 1) + 4 * (k8 & 1); k1 = k0 + 8; }
  else { k0 = 2 * DH * (k8 - 64) + 16; k1 = k0 + DH; }
  rows8(r, mk_rsrc(o ? wo : w_out, ok ? (o ? D : FH) * D * 2 : 0), n * 2, D * 2, k0, k1);
  const int j = n / 80, oc = n - j * 80, c = ncol(oc, 2);
  d0 = (o ? WS_WO : WS_WOUT) + ((j * (o ? 20 : 28) + (k8 >> 2)) * 5 + (c >> 4)) * 1024 + ((k8 & 3) * 16 + (c & 15)) * 16;
  d1 = d0 + (oc < 64 ? 1024 : 16);
}

constexpr int PT_QKV = 80 * 15;
constexpr int PT_BLOCKS = 400;
__global__ __launch_bounds__(256) void k_prep(const u16* __restrict__ c, const u16* __restrict__ w_mod, const u16* __restrict__ wqkv,
                                              const u16* __restrict__ wo, const u16* __restrict__ x, char* __restrict__ ws) {
  __shared__ float red[4][16];
  const unsigned WSCR = (unsigned)(uintptr_t)&red[0][0];
  const int tid = TID, lane = tid & 63, wv = ufl(tid >> 6), b = BID;
  TS_DECL; STAMP(0);
  if (b < 240) {
    const int cb = (b & 7) * 30 + (b >> 3);
    const int half = tid & 1, slot = tid >> 1;
    u32x4 v[5]; float sc[5];
#pragma unroll
    for (int i = 0; i < 5; ++i) {
      int k = slot + 128 * i;
      v[i] = __builtin_nontemporal_load((const u32x4*)(w_mod + (size_t)k * (6 * D) + cb * 16 + half * 8));
      float x = bf2f(c[k]);
      sc[i] = rbf(x * __builtin_amdgcn_rcpf(1.0f + __builtin_amdgcn_exp2f(-1.4426950408889634f * x)));
    }
    float a[8];
#pragma unroll
    for (int j = 0; j < 8; ++j) a[j] = 0.f;
#pragma unroll
    for (int i = 0; i < 5; ++i)
#pragma unroll
      for (int j = 0; j < 4; ++j) { a[2 * j] += sc[i] * bf_lo(v[i][j]); a[2 * j + 1] += sc[i] * bf_hi(v[i][j]); }
#pragma unroll
    for (int j = 0; j < 8; ++j) {
      a[j] = dpp_add<0x128>(a[j]); a[j] = dpp_add<0x124>(a[j]); a[j] = dpp_add<0x122>(a[j]);
      a[j] += __shfl_xor(a[j], 16); a[j] += __shfl_xor(a[j], 32);
    }
    if (lane < 2) {
#pragma unroll
      for (int j = 0; j < 8; ++j) red[wv][lane * 8 + j] = a[j];
    }
    __syncthreads();
    if (tid < 16) {
      float s = (red[0][tid] + red[1][tid]) + (red[2][tid] + red[3][tid]);
      int col = cb * 16 + tid, seg = col / D;
      s = rbf(s);
      if (seg == 1 || seg == 4) s = rbf(1.0f + s);
      ((float*)(ws + WS_MOD))[col] = s;
    }
    STAMP_END(ws, 0, 1);
    return;
  }
  if (b < 256) {
    u32x4* p = (u32x4*)(ws + WS_STAT) + (b - 240) * 512;
    p[tid] = u32x4{~0u, ~0u, ~0u, ~0u}; p[tid + 256] = u32x4{~0u, ~0u, ~0u, ~0u};
    if (tid < 64) ((u32x4*)(ws + WS_CTR))[(b - 240) * 64 + tid] = u32x4{0u, 0u, 0u, 0u};
    return;
  }
  int f = (b - 256) * 4 + wv;
  { const int i = ((b - 256) >> 3) * 4 + wv; if (i < 80) warm((const char*)x + (((b & 7) >> 1) * 80 + ufl(i)) * 8192, 1, lane, WSCR); }
  if (f < PT_QKV) {
    const int k8 = f / 15, n = (f - k8 * 15) * 128 + 2 * lane;
    unsigned r[8]; rows8(r, mk_rsrc(wqkv, D * 3 * D * 2), n * 2, 3 * D * 2, k8 * 8, k8 * 8 + 4);
    const int seg = n / D, cc = n - seg * D, j = cc / 80, o = cc - j * 80, hh = o / DH, d = o - hh * DH;
    int tile, rho;
    if (seg == 2) { tile = 10 + (o >> 4); rho = o & 15; }
    else if (d < 16) { const int e8 = 2 * hh + (d >> 3); tile = seg * 4 + 2 * (e8 >> 2) + ((d >> 2) & 1); rho = 4 * (e8 & 3) + (d & 3); }
    else { tile = 8 + seg; rho = 4 * hh + d - 16; }
    const int d0 = WS_WQKV + ((j * 15 + tile) * 20 + (k8 >> 2)) * 1024 + ((k8 & 3) * 16 + rho) * 16;
    put2<false>(ws, d0, d0 + 16, r);
    STAMP_END(ws, 0, 2);
    return;
  }
  unsigned r[8]; int d0, d1;
  tile_proj(r, d0, d1, wo, nullptr, f - PT_QKV, lane);
  put2<false>(ws, d0, d1, r);
}

constexpr int Q_GS = 81920, Q_LDS = Q_GS + 4 * 5120;
DEVI unsigned afrag_base(unsigned lds0, int lane, int odd) {
  const int r = lane & 15, g = lane >> 4;
  return lds0 + r * 128 + (((4 * odd + g) ^ (r & 7)) << 4);
}
constexpr int Q_AHEAD = 8;
template <bool VW>
DEVI void qkv_mma(char* __restrict__ ws, unsigned lds0, int wv, int pi, int j) {
  const int lane = TID & 63, r = lane & 15, g = lane >> 4;
  TS_DECL;
  const rsrc_t wr = mk_rsrc(ws + WS_WQKV + (j * 15 + 2 * wv) * 20 * 1024, wv < 7 ? 40 * 1024 : 20 * 1024);
  frag bw[20][2], a[2][4];
  auto ldb = [&](int ks) { if (ks < 20) { bw[ks][0] = bld16(wr, lane * 16, ks * 1024); bw[ks][1] = bld16(wr, lane * 16, (20 + ks) * 1024); } };
#pragma unroll
  for (int ks = 0; ks < Q_AHEAD; ++ks) ldb(ks);
  SBAR();
  f32x4 acc[2][4] = {};
  const unsigned a0 = afrag_base(lds0, lane, 0), a1 = afrag_base(lds0, lane, 1);
  STAMP(4);
  RAWBAR();
  STAMP(1);
#pragma unroll
  for (int mt = 0; mt < 4; ++mt) a[0][mt] = lds8(a0 + mt * 2048);
#pragma unroll
  for (int ks = 0; ks < 20; ++ks) {
    const int kl = ks + 1;
    if (kl < 20) {
      if ((kl & 3) == 0) RAWBAR();
#pragma unroll
      for (int mt = 0; mt < 4; ++mt) a[kl & 1][mt] = lds8(((kl & 1) ? a1 : a0) + (kl >> 1) * 8192 + mt * 2048);
    }
    SBAR();
#pragma unroll
    for (int mt = 0; mt < 4; ++mt)
#pragma unroll
      for (int t = 0; t < 2; ++t)
        acc[t][mt] = VW ? mfma16(a[ks & 1][mt], bw[ks][t], acc[t][mt]) : mfma16(bw[ks][t], a[ks & 1][mt], acc[t][mt]);
    SBAR();
    ldb(ks + Q_AHEAD);
    SBAR();
  }
  STAMP(5);
  const rsrc_t wsr = mk_rsrc(ws, WS_END);
  if (!VW) {
    const float qs = 0.22360679774997896f * 1.4426950408889634f;
    const int s0 = pi * 64 + prow(0, r);
    if (wv < 4) {
      const float sc = wv < 2 ? qs : 1.0f;
      const int voff = (8 * j + 4 * (wv & 1) + g) * (S * 16) + s0 * 16;
#pragma unroll
      for (int mt = 0; mt < 4; ++mt) bst16(wsr, voff, (wv < 2 ? WS_QA : WS_KA) + prow(mt, 0) * 16, pk8(acc[0][mt] * sc, acc[1][mt] * sc));
    } else {
      const int voff = (4 * j + g) * (S * 8) + s0 * 8;
#pragma unroll
      for (int mt = 0; mt < 4; ++mt) {
        bst8(wsr, voff, WS_QB + prow(mt, 0) * 8, pk4(acc[0][mt] * qs));
        bst8(wsr, voff, WS_KB + prow(mt, 0) * 8, pk4(acc[1][mt]));
      }
    }
  } else {
#pragma unroll
    for (int t = 0; t < 2; ++t) {
      if (t == 1 && wv == 7) break;
      const int o = 16 * (2 * wv + t - 10) + r, hh = o / DH, d = o - hh * DH;
      const int voff = (((4 * j + hh) * 256 + pi * 8 + g) * NV + d) * 16;
      bst16(wsr, voff, WS_VT, pk8(acc[t][0], acc[t][1]));
      bst16(wsr, voff, WS_VT + 4 * NV * 16, pk8(acc[t][2], acc[t][3]));
    }
  }
  STAMP(2);
  STAMP_END(ws, 1, 3);
}
__global__ __launch_bounds__(768) void k_qkv(const u16* __restrict__ x, char* __restrict__ ws) {
  extern __shared__ __attribute__((aligned(16))) char smem[];
  const int wv = ufl(TID >> 6), lane = TID & 63;
  const int pi = ((BID & 7) >> 1) * 8 + ((BID >> 3) & 7), j = (BID & 1) * 4 + (BID >> 6);
  const unsigned lds0 = (unsigned)(uintptr_t)smem;
  if (wv >= 9) { qkv_mma<true>(ws, lds0, wv - 4, pi, j); return; }
  if (wv >= 4) { qkv_mma<false>(ws, lds0, wv - 4, pi, j); return; }
  TS_DECL; STAMP(0);
  const int r = lane & 15, g = lane >> 4;
  u32x4 xv[4][5];
  {
    const rsrc_t xr = mk_rsrc(x + (size_t)(pi * 64 + wv * 16) * D, 16 * D * 2);
#pragma unroll
    for (int p = 0; p < 4; ++p)
#pragma unroll
      for (int i = 0; i < 5; ++i) xv[p][i] = bld16(xr, g * (D * 2) + r * 16, p * 4 * D * 2 + i * 256);
  }
#pragma unroll
  for (int i = 0; i < 5; ++i) glds16(ws + WS_MOD + i * 1024, lane * 16, lds0 + Q_GS + wv * 5120 + i * 1024);
  if (wv == 1) {
    const unsigned cv = (lane & 1) ? 0u : 0x3f803f80u;
    bst16(mk_rsrc(ws, WS_END), (((4 * j + g) * 256 + pi * 8 + (r >> 1)) * NV + DH + (lane & 1)) * 16, WS_VT, u32x4{cv, cv, cv, cv});
  }
  SBAR();
  STAMP(6);
  const unsigned gsa = lds0 + Q_GS + wv * 5120 + r * 32;
  f32x4 gs[2][4];
  auto ldgs = [&](int i) {
    gs[i & 1][0] = *(const LAS f32x4*)(gsa + i * 512); gs[i & 1][1] = *(const LAS f32x4*)(gsa + i * 512 + 16);
    gs[i & 1][2] = *(const LAS f32x4*)(gsa + 2560 + i * 512); gs[i & 1][3] = *(const LAS f32x4*)(gsa + 2560 + i * 512 + 16);
  };
  float rs[4], nb[4];
#pragma unroll
  for (int p = 0; p < 4; ++p) {
    float s1 = 0.f, s2 = 0.f;
#pragma unroll
    for (int i = 0; i < 5; ++i)
#pragma unroll
      for (int e = 0; e < 4; ++e) { const unsigned w = xv[p][i][e]; s1 = dot2(w, 0x3f803f80u, s1); s2 = dot2(w, w, s2); }
    const float mu = row16_sum(s1) * (1.0f / D);
    const float var = __builtin_fmaxf(row16_sum(s2) * (1.0f / D) - mu * mu, 0.f);
    rs[p] = __builtin_amdgcn_rsqf(var + 1e-5f);
    nb[p] = -mu * rs[p];
    PIN(rs[p]); PIN(nb[p]);
    if (p == 2) { WAITV(0); ldgs(0); }
  }
  STAMP(7);
  unsigned wa[2];
#pragma unroll
  for (int h = 0; h < 2; ++h) {
    const int ri = 8 * (wv & 1) + 4 * h + g;
    wa[h] = lds0 + (r >> 3) * 8192 + ((wv >> 1) * 32 + ri) * 128 + (((r & 7) ^ (ri & 7)) << 4);
  }
#pragma unroll
  for (int i = 0; i < 5; ++i) {
    if (i < 4) ldgs(i + 1);
#pragma unroll
    for (int p = 0; p < 4; ++p) {
      u32x4 o;
      PIN(xv[p][i]);
#pragma unroll
      for (int e = 0; e < 4; ++e) {
        const unsigned w = xv[p][i][e];
        const f32x4 &sv = gs[i & 1][e >> 1], &gv = gs[i & 1][2 + (e >> 1)];
        o[e] = pk2(__builtin_fmaf(__builtin_fmaf(bf_lo(w), rs[p], nb[p]), gv[2 * (e & 1)], sv[2 * (e & 1)]),
                   __builtin_fmaf(__builtin_fmaf(bf_hi(w), rs[p], nb[p]), gv[2 * (e & 1) + 1], sv[2 * (e & 1) + 1]));
      }
      *(LAS u32x4*)(wa[p >> 1] + i * 16384 + (p & 1) * 2048) = o;
    }
    BAR();
  }
  STAMP_END(ws, 1, 8);
}

template <int NS, int NF, class F>
DEVI void proj_mma(f32x4 (&c)[5], rsrc_t ar, int vt, int hioff, unsigned lds0, unsigned red, int hw, int lane, F&& late) {
  const int mh = hw >> 1, kp = hw & 1, vf = lane * 16 + mh * 2048;
  frag a[NS][2], b[2][5];
  auto lda = [&](int i) {
    if (i < NF) { a[i][0] = bld16c(ar, vf, i * 8192); a[i][1] = bld16c(ar, vf, i * 8192 + 1024); }
    else if (i < NS) { a[i][0] = bld16c(ar, vt, i * 8192); a[i][1] = bld16c(ar, vt, i * 8192 + hioff); }
  };
  auto upto = [](int ks) { return ks < 3 ? 6 + 2 * ks : ks + 9; };
#pragma unroll
  for (int i = 0; i < upto(0); ++i) { lda(i); SBAR(); }
  const unsigned bb = lds0 + kp * 5120 + lane * 16;
  f32x4 acc[2][5] = {};
#pragma unroll
  for (int t = 0; t < 5; ++t) b[0][t] = lds8(bb + t * 1024);
#pragma unroll
  for (int ks = 0; ks < NS; ++ks) {
    if (ks >= NF) {
      PIN(a[ks][0]); PIN(a[ks][1]);
      const frag lo = a[ks][0], hi = a[ks][1];
      a[ks][0] = frag{lo[0], lo[1], hi[0], hi[1]}; a[ks][1] = frag{lo[2], lo[3], hi[2], hi[3]};
    }
#pragma unroll
    for (int t = 0; t < 5; ++t) {
      if (ks + 1 < NS) b[(ks + 1) & 1][t] = lds8(bb + (ks + 1) * 10240 + t * 1024);
      SBAR();
      acc[0][t] = mfma16(b[ks & 1][t], a[ks][0], acc[0][t]); acc[1][t] = mfma16(b[ks & 1][t], a[ks][1], acc[1][t]);
      SBAR();
    }
#pragma unroll
    for (int i = upto(ks); i < upto(ks + 1); ++i) lda(i);
    if (ks == NS - 5) late();
    SBAR();
  }
#pragma unroll
  for (int t = 0; t < 5; ++t) {
    f32x4 lo = acc[0][t], hi = acc[1][t];
    PIN(lo); PIN(hi);
    *(LAS f32x4*)(red + hw * 5120 + t * 1024 + lane * 16) = kp ? lo : hi;
    c[t] = kp ? hi : lo;
  }
  BAR();
#pragma unroll
  for (int t = 0; t < 5; ++t) c[t] += *(const LAS f32x4*)(red + (hw ^ 1) * 5120 + t * 1024 + lane * 16);
}
DEVI void load_gate(u32x4 (&gv)[5], rsrc_t wsr, int g, int off) {
#pragma unroll
  for (int t = 0; t < 4; ++t) gv[t] = bld16(wsr, g * 32, off + (t >> 1) * 128 + (t & 1) * 16);
  gv[4] = bld16(wsr, g * 16, off + 256);
}

constexpr int AT_C = 512;
constexpr int AT_KB = 2 * AT_C * 16, AT_V = AT_KB + AT_C * 8, AT_SLOT = AT_V + (AT_C / 8) * NV * 16;
constexpr int AT_RED = 3 * AT_SLOT, M_PART = AT_RED + 8 * 3072, M_LN = M_PART + 4096, M_UV = M_LN + 512, AT_SCR = M_UV + 1792,
              AT_SYNC = AT_SCR + 256, AT_LDS = AT_SYNC + 64, M_X = 100 * 1024;
constexpr int CT_U = 0, CT_O = 32, CT_X1 = 40, CT_W = 72;
DEVI float max16(const f32x16& p) {
  float a = __builtin_fmaxf(__builtin_fmaxf(p[0], p[1]), p[2]), b = __builtin_fmaxf(__builtin_fmaxf(p[3], p[4]), p[5]);
  a = __builtin_fmaxf(__builtin_fmaxf(a, p[6]), p[7]); b = __builtin_fmaxf(__builtin_fmaxf(b, p[8]), p[9]);
  a = __builtin_fmaxf(__builtin_fmaxf(a, p[10]), p[11]); b = __builtin_fmaxf(__builtin_fmaxf(b, p[12]), p[13]);
  a = __builtin_fmaxf(__builtin_fmaxf(a, p[14]), p[15]);
  float m = __builtin_fmaxf(a, b);
  auto rr = __builtin_amdgcn_permlane32_swap(__float_as_uint(m), __float_as_uint(m), false, false);
  return __builtin_fmaxf(__uint_as_float(rr[0]), __uint_as_float(rr[1]));
}
DEVI frag pack8(const f32x16& p, int o) {
  return frag{pk2(p[o], p[o + 1]), pk2(p[o + 2], p[o + 3]), pk2(p[o + 4], p[o + 5]), pk2(p[o + 6], p[o + 7])};
}
DEVI int win_col(int n) {
  if (n >= FF) { const int e = n - FF; return (e >> 2) * 112 + 108 + (e & 3); }
  const int seg = n < FH, nn = n - (seg ? 0 : FH), j = nn / 108;
  return (2 * j + seg) * 112 + ncol(nn - j * 108, 3);
}
DEVI void tile_win(const u16* __restrict__ w_in, char* ws, int f, int lane) {
  const int k16 = f / 14, n = (f - k16 * 14) * 128 + 2 * lane;
  typedef __attribute__((address_space(4))) const float cfloat;
  cfloat* mod = (cfloat*)(ws + WS_MOD);
  unsigned r[2][8]; int k0[2], k1[2];
  const rsrc_t wr = mk_rsrc(w_in, D * FF * 2), wsr = mk_rsrc(ws, WS_END);
#pragma unroll
  for (int h = 0; h < 2; ++h) { bool ok; krows<80, 2>(k16 >> 1, 2 * (k16 & 1) + h, k0[h], k1[h], ok); rows8(r[h], wr, n < FF ? n * 2 : D * FF * 2, FF * 2, k0[h], k1[h]); }
  const int n0 = win_col(n), n1 = win_col(n + 1);
  const int db = WS_WIN + (k16 >> 1) * 1024 + (k16 & 1) * 512;
  const int d0 = db + (n0 >> 4) * 20480 + (n0 & 15) * 16, d1 = db + (n1 >> 4) * 20480 + (n1 & 15) * 16;
  float u0 = 0.f, u1 = 0.f, v0 = 0.f, v1 = 0.f;
#pragma unroll
  for (int h = 0; h < 2; ++h) {
    u32x4 c0, c1;
#pragma unroll
    for (int i = 0; i < 8; i += 2) {
      const int ka = (i < 4 ? k0[h] : k1[h] - 4) + i;
      const float ga = mod[4 * D + ka], gb = mod[4 * D + ka + 1], sa = mod[3 * D + ka], sb = mod[3 * D + ka + 1];
      const float a0 = bf_lo(r[h][i]), a1 = bf_hi(r[h][i]), b0 = bf_lo(r[h][i + 1]), b1 = bf_hi(r[h][i + 1]);
      const unsigned w0 = pk2(a0 * ga, b0 * gb), w1 = pk2(a1 * ga, b1 * gb);
      c0[i >> 1] = w0; c1[i >> 1] = w1;
      u0 = dot2(w0, 0x3f803f80u, u0); u1 = dot2(w1, 0x3f803f80u, u1);
      v0 = __builtin_fmaf(a0, sa, __builtin_fmaf(b0, sb, v0)); v1 = __builtin_fmaf(a1, sa, __builtin_fmaf(b1, sb, v1));
    }
    bst16c(wsr, d0, h * 256, c0); bst16c(wsr, d1, h * 256, c1);
  }
  bst16c(wsr, (k16 * 1792 + n) * 8, WS_UVP, __builtin_bit_cast(u32x4, f32x4{u0, v0, u1, v1}));
}
__global__ __launch_bounds__(768) void k_mid(const u16* __restrict__ w_in, const u16* __restrict__ w_out, const u16* __restrict__ x,
                                             const u16* __restrict__ w_sp, char* __restrict__ ws) {
  extern __shared__ __attribute__((aligned(16))) char smem[];
  const int tid = TID, lane = tid & 63, hw = ufl(tid >> 6), wv = hw - 4, r32 = lane & 31, hi = lane >> 5;
  const int head = (BID & 7) * 4 + ((BID >> 3) & 3), qb = BID >> 5;
  const unsigned lds0 = (unsigned)(uintptr_t)smem;
  LAS unsigned* sync = (LAS unsigned*)(lds0 + AT_SYNC);
  const int pi = (BID & 7) * 4 + ((BID >> 3) & 3), j = BID >> 5;
  GAS unsigned* ctr = (GAS unsigned*)(ws + WS_CTR);
  LAS f32x2* part = (LAS f32x2*)(lds0 + M_PART);
  LAS f32x2* lnp = (LAS f32x2*)(lds0 + M_LN);
  LAS f32x2* uvp = (LAS f32x2*)(lds0 + M_UV);
  const rsrc_t wsr = mk_rsrc(ws, WS_END);
  const char* KA = ws + WS_KA + head * (S * 32);
  const char* KB = ws + WS_KB + head * (S * 8);
  const char* VT = ws + WS_VT + head * (256 * NV * 16);
  TS_DECL; STAMP(0);
  if (hw < 4) {
    __builtin_amdgcn_s_setprio(2);
    auto stage = [&](int ch) {
      const unsigned sl = lds0 + ((ch + 2) % 3) * AT_SLOT;
#pragma unroll
      for (int i = 0; i < 2; ++i) {
        glds16(KA + ch * (AT_C * 16) + (hw + 4 * i) * 1024, lane * 16, sl + (hw + 4 * i) * 1024);
        glds16(KA + (S + ch * AT_C) * 16 + (hw + 4 * i) * 1024, lane * 16, sl + AT_C * 16 + (hw + 4 * i) * 1024);
      }
      glds16(KB + ch * (AT_C * 8) + hw * 1024, lane * 16, sl + AT_KB + hw * 1024);
#pragma unroll
      for (int i = 0; i < 6; ++i) {
        const int pc = min(hw + 4 * i, NV - 1);
        glds16(VT + ch * (NV * 1024) + pc * 1024, lane * 16, sl + AT_V + pc * 1024);
      }
    };
    auto landed = [&](int ch) { if (lane == 0) __hip_atomic_fetch_add(sync + ch, 1u, __ATOMIC_RELAXED, __HIP_MEMORY_SCOPE_WORKGROUP); };
    stage(0);
    if (tid < 16) sync[tid] = 0u;
    BAR();
    WAITV(0); landed(0);
    stage(1); stage(2);
    WAITV(11); landed(1);
    WAITV(0); landed(2);
    STAMP(6);
    const int f = BID * 4 + hw;
    const unsigned WSCR = lds0 + AT_SCR;
    if (f < 560) { tile_win(w_in, ws, f, lane); signal_done(sync + 7, 4u, ctr + CT_W * 32, lane); }
    else {
      unsigned r[2][8]; int d0[2], d1[2];
      const int f0 = f - 560, n = f0 < 96 ? 2 : 1;
#pragma unroll
      for (int i = 0; i < 2; ++i) if (i < n) tile_proj(r[i], d0[i], d1[i], nullptr, w_out, 400 + f0 + 464 * i, lane);
#pragma unroll
      for (int i = 0; i < 2; ++i) if (i < n) put2<true>(ws, d0[i], d1[i], r[i]);
    }
    STAMP(5);
    warm((const char*)w_sp + f * 8192, 1, lane, WSCR);
    wait_lds<8>(sync + 4, 8u);
    stage(3);
    WAITV(0); landed(3);
    wait_global(ctr + CT_W * 32, 140u);
    {
      const int o = 56 * (hw & 1) + lane, seg = hw >> 1;
      const int vo = (lane < 56 && o < 108) ? ((seg ? 0 : FH) + 108 * j + o) * 8 : WS_END;
      float u = 0.f, v = 0.f;
#pragma unroll
      for (int h = 0; h < 2; ++h) {
        u32x2 t[20];
#pragma unroll
        for (int i = 0; i < 20; ++i) t[i] = bld8c(wsr, vo, WS_UVP + (h * 20 + i) * 1792 * 8);
#pragma unroll
        for (int i = 0; i < 20; ++i) { u += __uint_as_float(t[i][0]); v += __uint_as_float(t[i][1]); }
      }
      if (lane < 56) uvp[seg * 112 + ncol(o, 3)] = f32x2{u, v};
    }
    {
      const int i = (BID >> 3) * 4 + hw;
      warm(ws + WS_WIN + i * 8192, 1, lane, WSCR); warm(ws + WS_WIN + (128 + i) * 8192, 1, lane, WSCR);
      if (i < 24) warm(ws + WS_WIN + (256 + i) * 8192, 1, lane, WSCR);
    }
    const char* wsrc = ws + WS_WO + (j * 100 + hw) * 1024;
    auto issue_w = [&](int n0, int n1) {
#pragma unroll
      for (int n = n0; n < n1; ++n) glds16(wsrc + n * 4096, lane * 16, lds0 + hw * 1024 + n * 4096);
    };
    wait_lds<8>(sync + 5, 8u); issue_w(0, 10);
    wait_lds<8>(sync + 6, 8u); issue_w(10, 21);
    STAMP(7);
    BAR();
    RAWBAR();
    issue_w(21, 25);
#pragma unroll
    for (int i = 0; i < 3; ++i) {
      const int pc = min(hw + 4 * i, 9), ch = pc * 64 + lane, row = ch / 10;
      glds16(x + (size_t)pi * 64 * D + 80 * j, prow(row >> 4, row & 15) * (D * 2) + (ch - row * 10) * 16, lds0 + M_X + pc * 1024);
    }
    WAITV(0);
    RAWBAR();
    STAMP(8);
    const int r = lane & 15, g = lane >> 4;
    const int xo = WS_X1 + pi * 81920;
    {
      u32x4 gv[5]; f32x4 c[5];
      proj_mma<10, 8>(c, mk_rsrc(ws + WS_O + pi * 81920 + (hw & 1) * 4096, 81920), g * 1024 + (hw >> 1) * 256 + r * 16, 512, lds0, lds0 + AT_RED, hw, lane,
                      [&]() { load_gate(gv, wsr, g, WS_MOD + 2 * D * 4 + 80 * j * 4); });
      const unsigned xa = lds0 + M_X + (hw * 16 + r) * 160;
      const u32x4 r0 = lds8(xa + g * 16), r1 = lds8(xa + 64 + g * 16); const u32x2 rt = lds4(xa + 128 + g * 8);
      bst16c(wsr, lane * 16, xo + 2 * j * 4096 + hw * 1024, gated(r0, gv[0], gv[1], c[0], c[1]));
      bst16c(wsr, lane * 16, xo + (2 * j + 1) * 4096 + hw * 1024, gated(r1, gv[2], gv[3], c[2], c[3]));
      bst8c(wsr, lane * 8, xo + 65536 + j * 2048 + hw * 512, pk4(up4(rt[0], rt[1]) + __builtin_bit_cast(f32x4, gv[4]) * c[4]));
      STAMP(9);
      signal_done(sync + 9, 4u, ctr + (CT_X1 + pi) * 32, lane);
    }
    RAWBAR();
    STAMP(10);
    {
      frag a[16]; u32x2 at[8];
      float s1 = 0.f, s2 = 0.f;
      auto ldx = [&](int gq) {
#pragma unroll
        for (int i = 0; i < 4; ++i) {
          if (gq < 4) a[4 * gq + i] = bld16c(wsr, lane * 16, xo + (4 * gq + i) * 4096 + hw * 1024);
          else { at[2 * i] = bld8c(wsr, lane * 8, xo + 65536 + i * 4096 + hw * 512); at[2 * i + 1] = bld8c(wsr, lane * 8, xo + 65536 + i * 4096 + 2048 + hw * 512); }
          SBAR();
        }
      };
      auto fr = [&](int i) { return i < 16 ? a[i] : frag{at[2 * i - 32][0], at[2 * i - 32][1], at[2 * i - 31][0], at[2 * i - 31][1]}; };
      ldx(0); ldx(1); ldx(2);
#pragma unroll
      for (int gq = 0; gq < 6; ++gq) {
        if (gq < 5) {
#pragma unroll
          for (int i = 4 * gq; i < 4 * gq + 4; ++i) *(LAS frag*)(lds0 + i * 4096 + hw * 1024 + lane * 16) = fr(i);
        }
        SBAR();
        if (gq > 0) {
#pragma unroll
          for (int i = 4 * gq - 4; i < 4 * gq; ++i) {
            const frag w = fr(i);
#pragma unroll
            for (int e = 0; e < 4; ++e) { const unsigned v = w[e]; s1 = dot2(v, 0x3f803f80u, s1); s2 = dot2(v, v, s2); }
          }
          PIN(s1); PIN(s2);
        }
        if (gq < 5) BAR();
        SBAR();
        if (gq == 0) { ldx(3); ldx(4); }
      }
      s1 += __shfl_xor(s1, 16); s2 += __shfl_xor(s2, 16); s1 += __shfl_xor(s1, 32); s2 += __shfl_xor(s2, 32);
      const float mu = s1 * (1.0f / D);
      const float rs = __builtin_amdgcn_rsqf(__builtin_fmaxf(s2 * (1.0f / D) - mu * mu, 0.f) + 1e-5f);
      if (g == 0) lnp[hw * 16 + r] = f32x2{rs, -mu * rs};
      BAR();
    }
    STAMP_END(ws, 2, 4);
    return;
  }
  const int qg = wv & 3, kh = wv >> 2;
  const int q0 = qb * 256 + qg * 64 + r32;
  const frag qaA = bld16(wsr, ((head * 2 + hi) * S + q0) * 16, WS_QA), qaB = bld16(wsr, ((head * 2 + hi) * S + q0) * 16, WS_QA + 512);
  u32x2 qbA = bld8(wsr, (head * S + q0) * 8, WS_QB), qbB = bld8(wsr, (head * S + q0) * 8, WS_QB + 256);
  RAWBAR();
  const int pk = (r32 & 0x13) | ((r32 & 4) << 1) | ((r32 & 8) >> 1);
  const unsigned s0 = lds0 + 2 * AT_SLOT, kz = s0 + hi * (AT_C * 16) + pk * 16, bz = s0 + AT_KB + pk * 8;
  const unsigned ka0 = hi * (AT_C * 16) + pk * 16 + kh * 4096, kb0 = AT_KB + pk * 8 + kh * 2048;
  const unsigned v0 = AT_V + (hi * NV + min(r32, NV - 1)) * 16 + kh * (32 * NV * 16);
  if (hi) { qbA = u32x2{0u, 0u}; qbB = u32x2{0u, 0u}; }
  PIN(qbA); PIN(qbB);
  wait_lds<1>(sync, 4u);
  STAMP(1);
  f32x16 oA = {}, oB = {}, nmA, nmB, sA, sB;
  frag ka, va, vb, plo, phi = {0u, 0u, 0u, 0u}; u32x2 kb;
  {
    ka = lds8(kz); kb = lds4(bz);
    sA = mfma32(ka, qaA, f32x16{}); sB = mfma32(ka, qaB, f32x16{});
    sA = mfma32k8(kb, qbA, sA); sB = mfma32k8(kb, qbB, sB);
    ka = lds8(s0 + ka0); kb = lds4(s0 + kb0); vb = lds8(s0 + v0); va = lds8(s0 + v0);
    const float mA = max16(sA), mB = max16(sB);
#pragma unroll
    for (int r = 0; r < 16; ++r) { nmA[r] = -mA; nmB[r] = -mB; }
    PIN(nmA); PIN(nmB);
    sA = mfma32(ka, qaA, nmA); sA = mfma32k8(kb, qbA, sA);
  }
#define PMASK 0x1111
#define PRIO(i) do { if ((((PMASK >> (i)) ^ (PMASK >> (((i) + 15) & 15))) & 1) != 0) { \
    asm volatile("s_cmp_eq_u32 %0, 0\n\ts_cbranch_scc1 1\n\ts_setprio %1" :: "s"(kh), "i"(((PMASK >> (i)) & 1) ? 0 : 1) : "scc"); SBAR(); } } while (0)
#define EX4(C, i, P, w) do { C[i] = __builtin_amdgcn_exp2f(C[i]); C[i + 1] = __builtin_amdgcn_exp2f(C[i + 1]); C[i + 2] = __builtin_amdgcn_exp2f(C[i + 2]); \
    C[i + 3] = __builtin_amdgcn_exp2f(C[i + 3]); P[w] = pk2(C[i], C[i + 1]); P[w + 1] = pk2(C[i + 2], C[i + 3]); PIN(P); SBAR(); } while (0)
  for (int ch = 0; ch < 4; ++ch) {
    const unsigned cur = lds0 + ((ch + 2) % 3) * AT_SLOT, nxt = lds0 + (ch % 3) * AT_SLOT;
    const unsigned kc = cur + ka0, kn = nxt + ka0, bc = cur + kb0, bn = nxt + kb0, vc = cur + v0, vn = nxt + v0;
#pragma unroll
    for (int t = 0; t < 8; ++t) {
      if (t == 7 && ch < 3) wait_lds<4>(sync + ch + 1, 4u);
      if (t == 1 && ch >= 1 && lane == 0) __hip_atomic_fetch_add(sync + 3 + ch, 1u, __ATOMIC_RELAXED, __HIP_MEMORY_SCOPE_WORKGROUP);
      PRIO(2 * t);
      sB = mfma32(ka, qaB, nmB); SBAR(); ka = lds8(t < 7 ? kc + (t + 1) * 512 : kn); SBAR(); EX4(sA, 0, plo, 0);
      oB = mfma32(vb, phi, oB); SBAR(); vb = lds8(vc + (4 * t + 2) * (NV * 16)); SBAR(); EX4(sA, 4, plo, 2);
      sB = mfma32k8(kb, qbB, sB); SBAR(); kb = lds4(t < 7 ? bc + (t + 1) * 256 : bn); SBAR(); EX4(sA, 8, phi, 0);
      oA = mfma32(va, plo, oA); SBAR(); EX4(sA, 12, phi, 2);
      PRIO(2 * t + 1);
      sA = mfma32(ka, qaA, nmA); SBAR(); EX4(sB, 0, plo, 0);
      oA = mfma32(vb, phi, oA); SBAR(); EX4(sB, 4, plo, 2);
      sA = mfma32k8(kb, qbA, sA); SBAR(); EX4(sB, 8, phi, 0);
      oB = mfma32(va, plo, oB); SBAR(); va = lds8(t < 7 ? vc + (4 * t + 4) * (NV * 16) : vn); SBAR(); EX4(sB, 12, phi, 2);
    }
  }
  oB = mfma32(vb, phi, oB);
#undef EX4
  STAMP(2);
  f32x16 o;
  {
    PIN(oA); PIN(oB);
    const unsigned rw = lds0 + AT_RED + wv * 3072 + lane * 16, rd = lds0 + AT_RED + (wv ^ 4) * 3072 + lane * 16;
#pragma unroll
    for (int i = 0; i < 3; ++i) {
      f32x4 w;
#pragma unroll
      for (int e = 0; e < 4; ++e) { w[e] = kh ? oA[4 * i + e] : oB[4 * i + e]; o[4 * i + e] = kh ? oB[4 * i + e] : oA[4 * i + e]; }
      *(LAS f32x4*)(rw + i * 1024) = w;
    }
    BAR();
#pragma unroll
    for (int i = 0; i < 3; ++i) {
      const f32x4 w = *(const LAS f32x4*)(rd + i * 1024);
#pragma unroll
      for (int e = 0; e < 4; ++e) o[4 * i + e] += w[e];
    }
  }
  float l = __shfl(o[8], r32 + 32);
  float chk = 0.f;
#pragma unroll
  for (int r = 0; r < 12; ++r) chk += __builtin_fabsf(o[r]);
  if (__builtin_expect(__any(!(chk < 3.0e38f)), 0)) {
    const frag qa = kh ? qaB : qaA; const u32x2 qb4 = kh ? qbB : qbA;
    o = f32x16{};
    float m = -INFINITY;
    for (int t = 0; t < 64; ++t) {
      const frag k1 = *(const frag*)(KA + (hi * S + t * 32 + pk) * 16);
      const u32x2 k2 = *(const u32x2*)(KB + (t * 32 + pk) * 8);
      const char* vp = VT + ((t * 4 + hi) * NV + min(r32, NV - 1)) * 16;
      const frag v1 = *(const frag*)vp, v2 = *(const frag*)(vp + 2 * NV * 16);
      f32x16 s = mfma32(k1, qa, f32x16{}); s = mfma32k8(k2, qb4, s);
      const float mn = __builtin_fmaxf(m, max16(s));
      const float al = __builtin_amdgcn_exp2f(m - mn);
      m = mn;
#pragma unroll
      for (int r = 0; r < 16; ++r) { s[r] = __builtin_amdgcn_exp2f(s[r] - mn); o[r] *= al; }
      o = mfma32(v1, pack8(s, 0), o); o = mfma32(v2, pack8(s, 8), o);
    }
    l = __shfl(o[8], r32 + 32);
  }
  const float rl = 1.0f / l;
  const int pos = pinv(32 * kh + r32);
  const int vo = (qb * 4 + qg) * (80 * 1024) + (pos >> 4) * 1024 + (pos & 15) * 16;
  bst16c(wsr, vo + hi * 256, WS_O + (head >> 1) * 4096 + (head & 1) * 512,
         u32x4{pk2(o[0] * rl, o[1] * rl), pk2(o[2] * rl, o[3] * rl), pk2(o[4] * rl, o[5] * rl), pk2(o[6] * rl, o[7] * rl)});
  if (!hi) bst8c(wsr, (qb * 4 + qg) * 81920 + kh * 256 + (pos & 15) * 16 + ((pos >> 4) & 1) * 8, WS_O + 65536 + head * 512,
                 u32x2{pk2(o[8] * rl, o[9] * rl), pk2(o[10] * rl, o[11] * rl)});
  __builtin_amdgcn_s_setprio(3);
  RAWBAR();
  signal_done(sync + 8, 8u, ctr + (CT_O + qb) * 32, lane);
  STAMP(3);
  GAS unsigned long long* gran = (GAS unsigned long long*)(ws + WS_STAT) + pi * 512;
  if (wv == 7) {
    wait_global(ctr + (CT_O + (pi >> 2)) * 32, 32u);
    RAWBAR(); RAWBAR();
    wait_global(ctr + (CT_X1 + pi) * 32, 8u);
#pragma unroll
    for (int i = 0; i < 7; ++i) RAWBAR();
    wait_lds<1>(sync + 10, 7u);
    float t1 = 0.f, t2 = 0.f;
#pragma unroll
    for (int w = 0; w < 7; ++w) { f32x2 p = part[w * 64 + lane]; t1 += p[0]; t2 += p[1]; }
    unsigned b1w = __float_as_uint(t1), b2w = __float_as_uint(t2);
    if (b2w == ~0u) b2w = 0x7fc00000u;
    __hip_atomic_store(gran + j * 64 + lane, ((unsigned long long)b2w << 32) | b1w, __ATOMIC_RELAXED, __HIP_MEMORY_SCOPE_AGENT);
    for (unsigned spins = 0;; ++spins) {
      bool ok = true; t1 = 0.f; t2 = 0.f;
#pragma unroll
      for (int w = 0; w < 8; ++w) {
        unsigned long long v = __hip_atomic_load(gran + w * 64 + lane, __ATOMIC_RELAXED, __HIP_MEMORY_SCOPE_AGENT);
        ok &= (unsigned)(v >> 32) != ~0u;
        t1 += __uint_as_float((unsigned)v); t2 += __uint_as_float((unsigned)(v >> 32));
      }
      if (__all(ok) || spins > (1u << 20)) break;
      __builtin_amdgcn_s_sleep(1);
    }
    const float mu = t1 * (1.0f / FH);
    const float rs = __builtin_amdgcn_rsqf(__builtin_fmaxf(t2 * (1.0f / FH) - mu * mu, 0.f) + 1e-5f);
    part[7 * 64 + lane] = f32x2{rs, -mu * rs};
    STAMP(14);
    BAR();
    STAMP_END(ws, 2, 4);
    return;
  }
  __builtin_amdgcn_s_setprio(0);
  const int r = lane & 15, g = lane >> 4;
  const rsrc_t wr = mk_rsrc(ws + WS_WIN + (j * 14 + wv) * 20 * 1024, 8 * 20 * 1024);
  frag bw[40], a[3][4];
  auto ldb = [&](int i) { if (i < 40) bw[i] = bld16c(wr, lane * 16, (i < 20 ? i : 120 + i) * 1024); };
  constexpr int WB = 18;
#pragma unroll
  for (int i = 0; i < 8; ++i) ldb(i);
  RAWBAR(); RAWBAR();
#pragma unroll
  for (int i = 8; i < WB; ++i) ldb(i);
  RAWBAR();
  f32x4 z2[4] = {}, z1[4] = {};
  const unsigned ab = lds0 + lane * 16;
#pragma unroll
  for (int ks = -2; ks < 40; ++ks) {
    const int kl = ks + 2;
    if (kl < 40) {
      if (kl < 20 && (kl & 3) == 0) RAWBAR();
#pragma unroll
      for (int mt = 0; mt < 4; ++mt) a[kl % 3][mt] = lds8(ab + (kl % 20) * 4096 + mt * 1024);
    }
    SBAR();
    if (ks < 0) continue;
#pragma unroll
    for (int mt = 0; mt < 4; ++mt) {
      if (ks < 20) z2[mt] = mfma16(a[ks % 3][mt], bw[ks], z2[mt]);
      else z1[mt] = mfma16(bw[ks], a[ks % 3][mt], z1[mt]);
    }
    SBAR();
    ldb(ks + WB);
    SBAR();
    if (ks == 19) {
      STAMP(11);
      RAWBAR();
      const f32x2 uv = uvp[wv * 16 + r];
      f32x2 st[4];
#pragma unroll
      for (int mt = 0; mt < 4; ++mt) {
        const f32x4 l0 = *(const LAS f32x4*)&lnp[mt * 16 + 4 * g], l1 = *(const LAS f32x4*)&lnp[mt * 16 + 4 * g + 2];
        f32x4 z = {gelu_tanh(__builtin_fmaf(z2[mt][0], l0[0], __builtin_fmaf(l0[1], uv[0], uv[1]))),
                   gelu_tanh(__builtin_fmaf(z2[mt][1], l0[2], __builtin_fmaf(l0[3], uv[0], uv[1]))),
                   gelu_tanh(__builtin_fmaf(z2[mt][2], l1[0], __builtin_fmaf(l1[1], uv[0], uv[1]))),
                   gelu_tanh(__builtin_fmaf(z2[mt][3], l1[2], __builtin_fmaf(l1[3], uv[0], uv[1])))};
        const u32x2 zb = pk4(z);
        z = up4(zb[0], zb[1]);
        z2[mt] = z;
        st[mt] = f32x2{quad4_sum(z), quad4_sum(z * z)};
      }
      if ((r & 3) == 0) {
#pragma unroll
        for (int mt = 0; mt < 4; ++mt) part[wv * 64 + mt * 16 + 4 * g + (r >> 2)] = st[mt];
      }
      asm volatile("s_waitcnt lgkmcnt(0)" ::: "memory");
      if (lane == 0) __hip_atomic_fetch_add(sync + 10, 1u, __ATOMIC_RELAXED, __HIP_MEMORY_SCOPE_WORKGROUP);
      STAMP(12);
    }
  }
  STAMP(13);
  {
    const f32x4 ua = *(const LAS f32x4*)&uvp[112 + wv * 16 + 4 * g], ub = *(const LAS f32x4*)&uvp[112 + wv * 16 + 4 * g + 2];
#pragma unroll
    for (int mt = 0; mt < 4; ++mt) {
      const f32x2 ln = lnp[mt * 16 + r];
      z1[mt][0] = gelu_tanh(__builtin_fmaf(z1[mt][0], ln[0], __builtin_fmaf(ln[1], ua[0], ua[1])));
      z1[mt][1] = gelu_tanh(__builtin_fmaf(z1[mt][1], ln[0], __builtin_fmaf(ln[1], ua[2], ua[3])));
      z1[mt][2] = gelu_tanh(__builtin_fmaf(z1[mt][2], ln[0], __builtin_fmaf(ln[1], ub[0], ub[1])));
      z1[mt][3] = gelu_tanh(__builtin_fmaf(z1[mt][3], ln[0], __builtin_fmaf(ln[1], ub[2], ub[3])));
    }
    const int zo = ((pi * 8 + j) * 7 + wv) * 2048 + lane * 16;
    bst16c(wsr, zo, WS_Z1, pk8(z1[0], z1[1])); bst16c(wsr, zo, WS_Z1 + 1024, pk8(z1[2], z1[3]));
  }
  BAR();
#pragma unroll
  for (int mt = 0; mt < 4; ++mt)
#pragma unroll
    for (int e = 0; e < 4; e += 2) {
      const f32x4 p = *(const LAS f32x4*)&part[7 * 64 + mt * 16 + 4 * g + e];
      z2[mt][e] = __builtin_fmaf(z2[mt][e], p[0], p[1]); z2[mt][e + 1] = __builtin_fmaf(z2[mt][e + 1], p[2], p[3]);
    }
  const int zo = ((j * 7 + wv) * 64 + pi * 2) * 1024 + lane * 16;
  bst16c(wsr, zo, WS_ZT, pk8(z2[0], z2[1])); bst16c(wsr, zo, WS_ZT + 1024, pk8(z2[2], z2[3]));
  STAMP_END(ws, 2, 15);
}

constexpr int G_W = 4, G_RING = 8, G_A = 65536, G_RED = G_A + G_RING * 8192, G_OUT = 140 * 1024, G_CNT = G_OUT + 10240, G_LDS = 160 * 1024;
template <int NT, bool FIRST, class F>
DEVI void sp_loop(f32x4 (&acc)[2][4], rsrc_t br, int voff, unsigned ab, int rot, F&& late TS_ARGS) {
  frag bw[G_W][2], a[2][4];
  auto issue = [&](int kb, int i) {
    const int o = kb < 32 ? ((kb + rot) & 31) * 2048 : (1 << 20);
    bw[i][0] = bld16(br, voff, o);
    if (NT > 1) bw[i][1] = bld16(br, voff + 65536, o);
  };
#pragma unroll
  for (int i = 0; i < G_W - (FIRST ? 1 : 0); ++i) issue(i, i);
  late();
  STAMP(5);
  RAWBAR();
  STAMP(7);
#pragma unroll
  for (int mt = 0; mt < 4; ++mt) a[0][mt] = lds8(ab + mt * 2048);
#pragma nounroll
  for (int k0 = 0; k0 < 32; k0 += G_RING) {
#pragma unroll
    for (int sl = 0; sl < G_RING; ++sl) {
      RAWBAR();
#pragma unroll
      for (int mt = 0; mt < 4; ++mt) a[(sl + 1) & 1][mt] = lds8(ab + ((sl + 1) % G_RING) * 8192 + mt * 2048);
      SBAR();
      if (FIRST) { issue(k0 + sl + G_W - 1, (sl + G_W - 1) % G_W); SBAR(); }
#pragma unroll
      for (int t = 0; t < NT; ++t)
#pragma unroll
        for (int mt = 0; mt < 4; ++mt) acc[t][mt] = mfma16(bw[sl % G_W][t], a[sl & 1][mt], acc[t][mt]);
      SBAR();
      if (!FIRST) { issue(k0 + sl + G_W, sl % G_W); SBAR(); }
    }
  }
  STAMP(10);
}
__global__ __launch_bounds__(768) void k_sp(const u16* __restrict__ w_sp, const u16* __restrict__ b_sp, u16* __restrict__ out, char* __restrict__ ws) {
  extern __shared__ __attribute__((aligned(16))) char smem[];
  const int tid = TID, lane = tid & 63, hw = ufl(tid >> 6), wv = hw - 4;
  const int xg = BID & 7, bi = BID >> 3;
  const int pi = (xg >> 1) * 8 + (bi & 7), j = (xg & 1) * 4 + (bi >> 3);
  const unsigned lds0 = (unsigned)(uintptr_t)smem;
  LAS unsigned* lcnt = (LAS unsigned*)(lds0 + G_LDS - 16);
  GAS unsigned* gcnt = (GAS unsigned*)(ws + WS_CTR) + (CT_U + pi) * 32;
#define ROT bi
  TS_DECL; STAMP(0);
  if (hw < 4) {
    const int rr = lane >> 3;
    const char* src = (const char*)w_sp + (size_t)(pi * 64 + prow(hw, 0)) * (S * 2);
    const unsigned sv0 = prow(0, rr) * (S * 2) + (((lane & 7) ^ rr) << 4), dst = lds0 + G_A + hw * 2048;
    auto issue = [&](int kb) {
      const char* s = src + ((kb + ROT) & 31) * 128;
      const unsigned d = dst + (kb & (G_RING - 1)) * 8192;
      glds16(s, sv0, d); glds16(s + 16 * (S * 2), sv0, d + 1024);
    };
    const char* wsrc = ws + WS_WOUT + (j * 140 + hw) * 1024;
    auto issue_w = [&](int n0, int n1) {
#pragma unroll
      for (int n = n0; n < n1; ++n) glds16(wsrc + n * 4096, lane * 16, lds0 + hw * 1024 + n * 4096);
    };
    constexpr int AH = 4;
    for (int kb = 0; kb < AH; ++kb) issue(kb);
    if (tid == 0) *lcnt = 0u;
#pragma nounroll
    for (int kb = 0; kb < 32 - AH; ++kb) { WAITV(2 * (AH - 1)); RAWBAR(); issue(kb + AH); }
    WAITV(6); RAWBAR(); issue_w(0, 4);
    WAITV(8); RAWBAR(); issue_w(4, 8);
    WAITV(10); RAWBAR(); issue_w(8, 12);
    WAITV(12); RAWBAR(); issue_w(12, 16);
    RAWBAR();
    STAMP(9);
    BAR();
    RAWBAR();
    issue_w(16, 35);
    const int r = lane & 15, g = lane >> 4;
    const rsrc_t wsr = mk_rsrc(ws, WS_END);
    u32x4 gv[5], rv[2]; u32x2 rt;
    STAMP(4);
    WAITV(0);
    RAWBAR();
    STAMP(6);
    f32x4 c[5];
    proj_mma<14, 12>(c, mk_rsrc(ws + WS_U + pi * (28 * 4096) + (hw & 1) * 4096, 28 * 4096), lane * 16 + (hw >> 1) * 1024, 2048, lds0, lds0 + G_OUT, hw, lane, [&]() {
      load_gate(gv, wsr, g, WS_MOD + 5 * D * 4 + 80 * j * 4);
      const int xo = WS_X1 + pi * 81920;
      rv[0] = bld16(wsr, lane * 16, xo + 2 * j * 4096 + hw * 1024); rv[1] = bld16(wsr, lane * 16, xo + (2 * j + 1) * 4096 + hw * 1024);
      rt = bld8(wsr, lane * 8, xo + 65536 + j * 2048 + hw * 512);
    });
    STAMP(8);
    const unsigned st = lds0 + G_OUT + (hw ^ 1) * 5120;
    const unsigned xa = st + r * 160;
#pragma unroll
    for (int p = 0; p < 2; ++p)
      *(LAS u32x4*)(xa + p * 64 + g * 16) = gated(rv[p], gv[2 * p], gv[2 * p + 1], c[2 * p], c[2 * p + 1]);
    *(LAS u32x2*)(xa + 128 + g * 8) = pk4(up4(rt[0], rt[1]) + __builtin_bit_cast(f32x4, gv[4]) * c[4]);
    const rsrc_t orr = mk_rsrc(out + (size_t)pi * 64 * D + 80 * j, 64 * D * 2 - 160 * j);
#pragma unroll
    for (int i = 0; i < 3; ++i) {
      const int ch = i * 64 + lane, row = ch / 10;
      if (ch < 160) bst16(orr, prow(hw, row) * (D * 2) + (ch - row * 10) * 16, 0, lds8(st + ch * 16));
    }
    STAMP_END(ws, 5, 11);
    return;
  }
  const int r = lane & 15, p = wv >> 1, kh = wv & 1;
  const rsrc_t br = mk_rsrc(ws + WS_ZT + (j * 7 + 2 * p) * 65536, 2 * 65536);
  const rsrc_t wsr = mk_rsrc(ws, WS_END);
  const int voff = lane * 16 + kh * 1024;
  const unsigned ab = afrag_base(lds0 + G_A, lane, kh);
  u32x4 z1v[2]; unsigned bsv[2];
  auto late = [&]() {
#pragma unroll
    for (int i = 0; i < 2; ++i) {
      bsv[i] = b_sp[pi * 64 + prow(2 * kh + i, r)];
      z1v[i] = bld16(wsr, ((pi * 8 + j) * 7 + min(2 * p + i, 6)) * 2048 + kh * 1024 + lane * 16, WS_Z1);
    }
  };
  f32x4 acc[2][4] = {};
  if (p < 2) sp_loop<2, true>(acc, br, voff, ab, ROT, late TS_PASS);
  else if (p == 2) sp_loop<2, false>(acc, br, voff, ab, ROT, late TS_PASS);
  else sp_loop<1, false>(acc, br, voff, ab, ROT, late TS_PASS);
  __builtin_amdgcn_s_setprio(2);
  const unsigned rw = lds0 + G_RED + wv * 4096 + lane * 16, rd = lds0 + G_RED + (wv ^ 1) * 4096 + lane * 16;
  f32x4 c[2][2];
#pragma unroll
  for (int t = 0; t < 2; ++t)
#pragma unroll
    for (int i = 0; i < 2; ++i) {
      f32x4 lo = acc[t][i], hi = acc[t][2 + i];
      PIN(lo); PIN(hi);
      if (t == 0 || p < 3) *(LAS f32x4*)(rw + (t * 2 + i) * 1024) = kh ? lo : hi;
      c[t][i] = kh ? hi : lo;
    }
  BAR();
#pragma unroll
  for (int t = 0; t < 2; ++t)
#pragma unroll
    for (int i = 0; i < 2; ++i) c[t][i] += *(const LAS f32x4*)(rd + (t * 2 + i) * 1024);
  STAMP(2);
  f32x4 y[2][2];
#pragma unroll
  for (int i = 0; i < 2; ++i) {
    const float bs = bf_lo(bsv[i]);
    y[0][i] = up4(z1v[0][2 * i], z1v[0][2 * i + 1]) * (c[0][i] + bs); y[1][i] = up4(z1v[1][2 * i], z1v[1][2 * i + 1]) * (c[1][i] + bs);
  }
  if (p < 3) {
    bst16c(wsr, (pi * 28 + 3 * j + p) * 4096 + kh * 2048 + lane * 16, WS_U, pkI(y[0][0], y[1][0]));
    bst16c(wsr, (pi * 28 + 3 * j + p) * 4096 + kh * 2048 + lane * 16, WS_U + 1024, pkI(y[0][1], y[1][1]));
  } else {
    bst16c(wsr, (pi * 28 + 24) * 4096 + (2 * j + kh) * 1024 + lane * 16, WS_U, pk8(y[0][0], y[0][1]));
  }
  RAWBAR();
  WAITV(0); STAMP(1);
  signal_done(lcnt, 8u, gcnt, lane);
  if (wv == 0) wait_global(gcnt, 8u);
  STAMP_END(ws, 5, 3);
}

#if defined(__HIP_DEVICE_COMPILE__)
#pragma clang attribute pop
#endif
extern "C" void kernel_launch(void* const* d_in, const int* in_sizes, int n_in, void* d_out, int out_size, void* d_ws,
                              size_t ws_size, hipStream_t stream) {
  const u16* x = (const u16*)d_in[0];
  const u16* wqkv = (const u16*)d_in[1];
  const u16* wo = (const u16*)d_in[2];
  const u16* w_in = (const u16*)d_in[4];
  const u16* w_sp = (const u16*)d_in[5];
  const u16* b_sp = (const u16*)d_in[6];
  const u16* w_out = (const u16*)d_in[7];
  const u16* c = (const u16*)d_in[8];
  const u16* w_mod = (const u16*)d_in[9];
  char* ws = (char*)d_ws;
  (void)hipFuncSetAttribute((const void*)k_qkv, hipFuncAttributeMaxDynamicSharedMemorySize, Q_LDS);
  (void)hipFuncSetAttribute((const void*)k_mid, hipFuncAttributeMaxDynamicSharedMemorySize, AT_LDS);
  (void)hipFuncSetAttribute((const void*)k_sp, hipFuncAttributeMaxDynamicSharedMemorySize, G_LDS);
#define REP
  REP k_prep<<<256 + PT_BLOCKS, 256, 0, stream>>>(c, w_mod, wqkv, wo, x, ws);
  REP k_qkv<<<256, 768, Q_LDS, stream>>>(x, ws);
  REP k_mid<<<256, 768, AT_LDS, stream>>>(w_in, w_out, x, w_sp, ws);
  REP k_sp<<<256, 768, G_LDS, stream>>>(w_sp, b_sp, (u16*)d_out, ws);
}
```

```cpp
#if defined(__HIP_DEVICE_COMPILE__)
#pragma clang attribute push(__attribute__((target("no-packed-fp32-ops"))), apply_to = function)
#endif
#include <hip/hip_runtime.h>
#include <hip/hip_bf16.h>
#include <stdint.h>

typedef unsigned short u16;
typedef __attribute__((ext_vector_type(8))) __bf16 bf16v8;
typedef __attribute__((ext_vector_type(2))) __bf16 bf16x2;
typedef __attribute__((ext_vector_type(4))) short s16x4;
typedef __attribute__((ext_vector_type(2))) float f32x2;
typedef __attribute__((ext_vector_type(4))) float f32x4;
typedef __attribute__((ext_vector_type(16))) float f32x16;
typedef __attribute__((ext_vector_type(4))) unsigned u32x4;
typedef __attribute__((ext_vector_type(2))) unsigned u32x2;
typedef u32x4 frag;
#define LAS __attribute__((address_space(3)))
#define GAS __attribute__((address_space(1)))
#define DEVI __device__ __forceinline__
#define TID ((int)__builtin_amdgcn_workitem_id_x())
#define BID ((int)__builtin_amdgcn_workgroup_id_x())

constexpr int S = 2048, D = 640, NH = 32, DH = 20, FF = 1728, FH = 864;
constexpr int NV = 22;

constexpr int WS_MOD  = 0;
constexpr int WS_UV   = 16384;
constexpr int WS_STAT = 32768;
constexpr int WS_CTR  = WS_STAT + 32 * 8 * 64 * 8;
constexpr int WS_XST  = WS_CTR + 128 * 128;
constexpr int WS_UVP  = WS_XST + 32 * 8 * 64 * 8;
constexpr int WS_WQKV = WS_UVP + 40 * 1792 * 8;
constexpr int WS_WO   = WS_WQKV + 8 * 15 * 20 * 1024;
constexpr int WS_WIN  = WS_WO + 40 * 20 * 1024;
constexpr int WS_WOUT = WS_WIN + 8 * 14 * 20 * 1024;
constexpr int WS_QA   = WS_WOUT + 40 * 28 * 1024;
constexpr int WS_QB   = WS_QA + NH * S * 32;
constexpr int WS_KA   = WS_QB + NH * S * 8;
constexpr int WS_KB   = WS_KA + NH * S * 32;
constexpr int WS_VT   = WS_KB + NH * S * 8;
constexpr int WS_O    = WS_VT + NH * 256 * NV * 16;
constexpr int WS_X1   = WS_O + S * D * 2;
constexpr int WS_ZT   = WS_X1 + S * D * 2;
constexpr int WS_Z1   = WS_ZT + 8 * 7 * 64 * 1024;
constexpr int WS_U    = WS_Z1 + 32 * 8 * 7 * 2 * 1024;
constexpr int WS_END  = WS_U + 32 * 28 * 4 * 1024;

#define TS_DECL
#define TS_ARGS
#define TS_PASS
#define STAMP(s)
#define STAMP_END(ws, k, s)

DEVI float bf_lo(unsigned w) { return __uint_as_float(w << 16); }
DEVI float bf_hi(unsigned w) { return __uint_as_float(w & 0xffff0000u); }
DEVI float bf2f(u16 h) { return __uint_as_float((unsigned)h << 16); }
DEVI unsigned pk2(float a, float b) { bf16x2 v = {(__bf16)a, (__bf16)b}; return __builtin_bit_cast(unsigned, v); }
DEVI float rbf(float a) { return bf_lo(pk2(a, a)); }
DEVI u32x2 pk4(const f32x4& a) { return u32x2{pk2(a[0], a[1]), pk2(a[2], a[3])}; }
DEVI u32x4 pk8(const f32x4& a, const f32x4& b) { return u32x4{pk2(a[0], a[1]), pk2(a[2], a[3]), pk2(b[0], b[1]), pk2(b[2], b[3])}; }
DEVI u32x4 pkI(const f32x4& a, const f32x4& b) { return u32x4{pk2(a[0], b[0]), pk2(a[1], b[1]), pk2(a[2], b[2]), pk2(a[3], b[3])}; }
DEVI f32x4 upE(const u32x4& w) { return f32x4{bf_lo(w[0]), bf_lo(w[1]), bf_lo(w[2]), bf_lo(w[3])}; }
DEVI f32x4 upO(const u32x4& w) { return f32x4{bf_hi(w[0]), bf_hi(w[1]), bf_hi(w[2]), bf_hi(w[3])}; }
DEVI f32x4 up4(unsigned a, unsigned b) { return f32x4{bf_lo(a), bf_hi(a), bf_lo(b), bf_hi(b)}; }
DEVI float dot2(unsigned a, unsigned b, float c) { return __builtin_amdgcn_fdot2_f32_bf16(__builtin_bit_cast(bf16x2, a), __builtin_bit_cast(bf16x2, b), c, false); }
DEVI f32x4 mfma16(frag a, frag b, f32x4 c) { return __builtin_amdgcn_mfma_f32_16x16x32_bf16(__builtin_bit_cast(bf16v8, a), __builtin_bit_cast(bf16v8, b), c, 0, 0, 0); }
DEVI f32x16 mfma32(frag a, frag b, f32x16 c) { return __builtin_amdgcn_mfma_f32_32x32x16_bf16(__builtin_bit_cast(bf16v8, a), __builtin_bit_cast(bf16v8, b), c, 0, 0, 0); }
DEVI f32x16 mfma32k8(u32x2 a, u32x2 b, f32x16 c) { return __builtin_amdgcn_mfma_f32_32x32x8bf16_1k(__builtin_bit_cast(s16x4, a), __builtin_bit_cast(s16x4, b), c, 0, 0, 0); }
DEVI frag lds8(unsigned a) { return *(const LAS frag*)a; }
DEVI u32x2 lds4(unsigned a) { return *(const LAS u32x2*)a; }
typedef __amdgpu_buffer_rsrc_t rsrc_t;
DEVI rsrc_t mk_rsrc(const void* p, int bytes) { return __builtin_amdgcn_make_buffer_rsrc((void*)p, 0, bytes, 0x00020000); }
DEVI frag bld16(rsrc_t r, int voff, int soff) { return __builtin_amdgcn_raw_buffer_load_b128(r, voff, soff, 0); }
DEVI u32x2 bld8(rsrc_t r, int voff, int soff) { return __builtin_amdgcn_raw_buffer_load_b64(r, voff, soff, 0); }
#define ST_AUX 0
DEVI void bst16(rsrc_t r, int voff, int soff, u32x4 v) { __builtin_amdgcn_raw_buffer_store_b128(v, r, voff + soff, 0, ST_AUX); }
DEVI void bst8(rsrc_t r, int voff, int soff, u32x2 v) { __builtin_amdgcn_raw_buffer_store_b64(v, r, voff, soff, ST_AUX); }
DEVI void bst16c(rsrc_t r, int voff, int soff, u32x4 v) { __builtin_amdgcn_raw_buffer_store_b128(v, r, voff + soff, 0, 16); }
DEVI void bst8c(rsrc_t r, int voff, int soff, u32x2 v) { __builtin_amdgcn_raw_buffer_store_b64(v, r, voff, soff, 16); }
DEVI u32x2 bld8c(rsrc_t r, int voff, int soff) { return __builtin_amdgcn_raw_buffer_load_b64(r, voff, soff, 16); }
DEVI frag bld16c(rsrc_t r, int voff, int soff) { return __builtin_amdgcn_raw_buffer_load_b128(r, voff, soff, 16); }
DEVI void glds16(const void* base, unsigned voff, unsigned lds_base) {
  unsigned sv;
  asm volatile("s_mov_b32 %0, m0\n\ts_mov_b32 m0, %3\n\ts_nop 0\n\tglobal_load_lds_dwordx4 %1, %2\n\ts_mov_b32 m0, %0"
               : "=&s"(sv) : "v"(voff), "s"(base), "s"(lds_base) : "memory");
}
DEVI void warm(const void* p, int n8k, int lane, unsigned scratch) {
  for (int i = 0; i < n8k; ++i) {
    unsigned sv;
    asm volatile("s_mov_b32 %0, m0\n\ts_mov_b32 m0, %3\n\ts_nop 0\n\tglobal_load_lds_dword %1, %2\n\ts_mov_b32 m0, %0"
                 : "=&s"(sv) : "v"(lane * 128 + i * 8192), "s"(p), "s"(scratch) : "memory");
  }
}
#define WAITV(n) asm volatile("s_waitcnt vmcnt(%0)" ::"i"(n) : "memory")
#define BAR() asm volatile("s_waitcnt lgkmcnt(0)\n\ts_barrier" ::: "memory")
#define RAWBAR() asm volatile("s_barrier" ::: "memory")
#define SBAR() __builtin_amdgcn_sched_barrier(0)
#define PIN(x) asm volatile("" : "+v"(x))
DEVI int ufl(int x) { return __builtin_amdgcn_readfirstlane(x); }
DEVI void signal_done(LAS unsigned* lcnt, unsigned n, GAS unsigned* gcnt, int lane) {
  WAITV(0);
  if (lane == 0 && __hip_atomic_fetch_add(lcnt, 1u, __ATOMIC_RELAXED, __HIP_MEMORY_SCOPE_WORKGROUP) == n - 1)
    __hip_atomic_fetch_add(gcnt, 1u, __ATOMIC_RELAXED, __HIP_MEMORY_SCOPE_AGENT);
}
template <int NAP> DEVI void wait_lds(LAS unsigned* lcnt, unsigned n) {
  for (unsigned spins = 0; (unsigned)ufl(__hip_atomic_load(lcnt, __ATOMIC_RELAXED, __HIP_MEMORY_SCOPE_WORKGROUP)) < n && spins < (1u << 22); ++spins) __builtin_amdgcn_s_sleep(NAP);
  asm volatile("" ::: "memory");
}
DEVI void wait_global(GAS unsigned* gcnt, unsigned n) {
  for (unsigned spins = 0; __hip_atomic_load(gcnt, __ATOMIC_RELAXED, __HIP_MEMORY_SCOPE_AGENT) < n && spins < (1u << 21); ++spins) __builtin_amdgcn_s_sleep(4);
  asm volatile("" ::: "memory");
}
template <int CTRL> DEVI float dpp_add(float v) {
  return v + __builtin_bit_cast(float, __builtin_amdgcn_update_dpp(0, __builtin_bit_cast(int, v), CTRL, 0xf, 0xf, true));
}
DEVI float row16_sum(float v) {
  v = dpp_add<0x128>(v); v = dpp_add<0x124>(v); v = dpp_add<0x122>(v); v = dpp_add<0x121>(v);
  return v;
}
DEVI float quad4_sum(const f32x4& v) {
  float x, y, o;
  asm("s_nop 1\n\t"
      "v_add_f32_dpp %0, %3, %3 row_ror:8 row_mask:0xf bank_mask:0x3\n\t"
      "v_add_f32_dpp %1, %4, %4 row_ror:8 row_mask:0xf bank_mask:0x3\n\t"
      "v_add_f32_dpp %0, %5, %5 row_ror:8 row_mask:0xf bank_mask:0xc\n\t"
      "v_add_f32_dpp %1, %6, %6 row_ror:8 row_mask:0xf bank_mask:0xc\n\t"
      "s_nop 0\n\t"
      "v_add_f32_dpp %2, %0, %0 row_half_mirror row_mask:0xf bank_mask:0x5\n\t"
      "v_add_f32_dpp %2, %1, %1 row_half_mirror row_mask:0xf bank_mask:0xa\n\t"
      "s_nop 1\n\t"
      "v_add_f32_dpp %2, %2, %2 quad_perm:[1,0,3,2] row_mask:0xf bank_mask:0xf\n\t"
      "s_nop 1\n\t"
      "v_add_f32_dpp %2, %2, %2 quad_perm:[2,3,0,1] row_mask:0xf bank_mask:0xf"
      : "=&v"(x), "=&v"(y), "=&v"(o) : "v"(v[0]), "v"(v[1]), "v"(v[2]), "v"(v[3]));
  return o;
}
DEVI float gelu_tanh(float x) {
  const float c1 = -2.0f * 0.7978845608028654f * 1.4426950408889634f, c3 = c1 * 0.044715f;
  float e = __builtin_amdgcn_exp2f(x * __builtin_fmaf(x * x, c3, c1));
  return x * __builtin_amdgcn_rcpf(1.0f + e);
}
DEVI u32x4 gated(const u32x4& res, const u32x4& g0, const u32x4& g1, const f32x4& ce, const f32x4& co) {
  const f32x4 a = __builtin_bit_cast(f32x4, g0), b = __builtin_bit_cast(f32x4, g1);
  return pkI(upE(res) + f32x4{a[0], a[2], b[0], b[2]} * ce, upO(res) + f32x4{a[1], a[3], b[1], b[3]} * co);
}
DEVI int prow(int mt, int r) { return 32 * (mt >> 1) + 8 * (r >> 2) + 4 * (mt & 1) + (r & 3); }
DEVI int pinv(int o) { return (o & ~31) | ((o & 4) << 2) | ((o >> 1) & 12) | (o & 3); }

DEVI void rows8(unsigned (&r)[8], rsrc_t w, int voff, int pitch, int k0, int k1) {
#pragma unroll
  for (int i = 0; i < 8; ++i) r[i] = __builtin_amdgcn_raw_buffer_load_b32(w, voff, ((i < 4 ? k0 : k1 - 4) + i) * pitch, 2);
}
template <bool WT> DEVI void put2(char* ws, int d0, int d1, const unsigned (&r)[8]) {
  u32x4 c0, c1;
#pragma unroll
  for (int i = 0; i < 4; ++i) {
    c0[i] = __builtin_amdgcn_perm(r[2 * i + 1], r[2 * i], 0x05040100u);
    c1[i] = __builtin_amdgcn_perm(r[2 * i + 1], r[2 * i], 0x07060302u);
  }
  const rsrc_t wsr = mk_rsrc(ws, WS_END);
  if (WT) { bst16c(wsr, d0, 0, c0); bst16c(wsr, d1, 0, c1); } else { bst16(wsr, d0, 0, c0); bst16(wsr, d1, 0, c1); }
}
DEVI int ncol(int o, int npair) { return o < 32 * npair ? (o & ~31) | ((o & 1) << 4) | ((o >> 1) & 15) : o; }
template <int W, int NP> DEVI void krows(int ks, int g, int& k0, int& k1, bool& ok) {
  ok = true;
  if (ks < 8 * NP) { const int j = ks / NP; k0 = W * j + 32 * (ks - j * NP) + 8 * g; k1 = k0 + 4; }
  else { k0 = W * 2 * (ks - 8 * NP) + 32 * NP + 4 * g; k1 = k0 + W; ok = 32 * NP + 4 * g < W; }
}
DEVI void tile_proj(unsigned (&r)[8], int& d0, int& d1, const u16* __restrict__ wo, const u16* __restrict__ w_out, int f, int lane) {
  const bool o = f < 400;
  if (!o) f -= 400;
  const int k8 = f / 5, n = (f - k8 * 5) * 128 + 2 * lane;
  int k0, k1; bool ok = true;
  if (!o) krows<108, 3>(k8 >> 2, k8 & 3, k0, k1, ok);
  else if (k8 < 64) { k0 = DH * (k8 >> 1) + 4 * (k8 & 1); k1 = k0 + 8; }
  else { k0 = 2 * DH * (k8 - 64) + 16; k1 = k0 + DH; }
  rows8(r, mk_rsrc(o ? wo : w_out, ok ? (o ? D : FH) * D * 2 : 0), n * 2, D * 2, k0, k1);
  const int j = n / 80, oc = n - j * 80, c = ncol(oc, 2);
  d0 = (o ? WS_WO : WS_WOUT) + ((j * (o ? 20 : 28) + (k8 >> 2)) * 5 + (c >> 4)) * 1024 + ((k8 & 3) * 16 + (c & 15)) * 16;
  d1 = d0 + (oc < 64 ? 1024 : 16);
}

constexpr int PT_QKV = 80 * 15;
constexpr int PT_BLOCKS = 544;
__global__ __launch_bounds__(256) void k_prep(const u16* __restrict__ c, const u16* __restrict__ w_mod, const u16* __restrict__ wqkv,
                                              const u16* __restrict__ wo, const u16* __restrict__ w_out, const u16* __restrict__ x, char* __restrict__ ws) {
  __shared__ float red[4][16];
  const unsigned WSCR = (unsigned)(uintptr_t)&red[0][0];
  const int tid = TID, lane = tid & 63, wv = ufl(tid >> 6), b = BID;
  TS_DECL; STAMP(0);
  if (b < 240) {
    const int cb = (b & 7) * 30 + (b >> 3);
    const int half = tid & 1, slot = tid >> 1;
    u32x4 v[5]; float sc[5];
#pragma unroll
    for (int i = 0; i < 5; ++i) {
      int k = slot + 128 * i;
      v[i] = __builtin_nontemporal_load((const u32x4*)(w_mod + (size_t)k * (6 * D) + cb * 16 + half * 8));
      float x = bf2f(c[k]);
      sc[i] = rbf(x * __builtin_amdgcn_rcpf(1.0f + __builtin_amdgcn_exp2f(-1.4426950408889634f * x)));
    }
    float a[8];
#pragma unroll
    for (int j = 0; j < 8; ++j) a[j] = 0.f;
#pragma unroll
    for (int i = 0; i < 5; ++i)
#pragma unroll
      for (int j = 0; j < 4; ++j) { a[2 * j] += sc[i] * bf_lo(v[i][j]); a[2 * j + 1] += sc[i] * bf_hi(v[i][j]); }
#pragma unroll
    for (int j = 0; j < 8; ++j) {
      a[j] = dpp_add<0x128>(a[j]); a[j] = dpp_add<0x124>(a[j]); a[j] = dpp_add<0x122>(a[j]);
      a[j] += __shfl_xor(a[j], 16); a[j] += __shfl_xor(a[j], 32);
    }
    if (lane < 2) {
#pragma unroll
      for (int j = 0; j < 8; ++j) red[wv][lane * 8 + j] = a[j];
    }
    __syncthreads();
    if (tid < 16) {
      float s = (red[0][tid] + red[1][tid]) + (red[2][tid] + red[3][tid]);
      int col = cb * 16 + tid, seg = col / D;
      s = rbf(s);
      if (seg == 1 || seg == 4) s = rbf(1.0f + s);
      ((float*)(ws + WS_MOD))[col] = s;
    }
    STAMP_END(ws, 0, 1);
    return;
  }
  if (b < 256) {
    u32x4* p = (u32x4*)(ws + WS_STAT) + (b - 240) * 512;
    p[tid] = u32x4{~0u, ~0u, ~0u, ~0u}; p[tid + 256] = u32x4{~0u, ~0u, ~0u, ~0u};
    if (tid < 64) ((u32x4*)(ws + WS_CTR))[(b - 240) * 64 + tid] = u32x4{0u, 0u, 0u, 0u};
    return;
  }
  int f = (b - 256) * 4 + wv;
  { const int i = ((b - 256) >> 3) * 4 + wv; if (i < 80) warm((const char*)x + (((b & 7) >> 1) * 80 + ufl(i)) * 8192, 1, lane, WSCR); }
  if (f < PT_QKV) {
    const int k8 = f / 15, n = (f - k8 * 15) * 128 + 2 * lane;
    unsigned r[8]; rows8(r, mk_rsrc(wqkv, D * 3 * D * 2), n * 2, 3 * D * 2, k8 * 8, k8 * 8 + 4);
    const int seg = n / D, cc = n - seg * D, j = cc / 80, o = cc - j * 80, hh = o / DH, d = o - hh * DH;
    int tile, rho;
    if (seg == 2) { tile = 10 + (o >> 4); rho = o & 15; }
    else if (d < 16) { const int e8 = 2 * hh + (d >> 3); tile = seg * 4 + 2 * (e8 >> 2) + ((d >> 2) & 1); rho = 4 * (e8 & 3) + (d & 3); }
    else { tile = 8 + seg; rho = 4 * hh + d - 16; }
    const int d0 = WS_WQKV + ((j * 15 + tile) * 20 + (k8 >> 2)) * 1024 + ((k8 & 3) * 16 + rho) * 16;
    put2<false>(ws, d0, d0 + 16, r);
    STAMP_END(ws, 0, 2);
    return;
  }
  if (f >= PT_QKV + 960) return;
  unsigned r[8]; int d0, d1;
  tile_proj(r, d0, d1, wo, w_out, f - PT_QKV, lane);
  put2<false>(ws, d0, d1, r);
}

constexpr int Q_GS = 81920, Q_LDS = Q_GS + 4 * 5120;
DEVI unsigned afrag_base(unsigned lds0, int lane, int odd) {
  const int r = lane & 15, g = lane >> 4;
  return lds0 + r * 128 + (((4 * odd + g) ^ (r & 7)) << 4);
}
constexpr int Q_AHEAD = 8;
template <bool VW>
DEVI void qkv_mma(char* __restrict__ ws, unsigned lds0, int wv, int pi, int j) {
  const int lane = TID & 63, r = lane & 15, g = lane >> 4;
  TS_DECL;
  const rsrc_t wr = mk_rsrc(ws + WS_WQKV + (j * 15 + 2 * wv) * 20 * 1024, wv < 7 ? 40 * 1024 : 20 * 1024);
  frag bw[20][2], a[2][4];
  auto ldb = [&](int ks) { if (ks < 20) { bw[ks][0] = bld16(wr, lane * 16, ks * 1024); bw[ks][1] = bld16(wr, lane * 16, (20 + ks) * 1024); } };
#pragma unroll
  for (int ks = 0; ks < Q_AHEAD; ++ks) ldb(ks);
  SBAR();
  f32x4 acc[2][4] = {};
  const unsigned a0 = afrag_base(lds0, lane, 0), a1 = afrag_base(lds0, lane, 1);
  STAMP(4);
  RAWBAR();
  STAMP(1);
#pragma unroll
  for (int mt = 0; mt < 4; ++mt) a[0][mt] = lds8(a0 + mt * 2048);
#pragma unroll
  for (int ks = 0; ks < 20; ++ks) {
    const int kl = ks + 1;
    if (kl < 20) {
      if ((kl & 3) == 0) RAWBAR();
#pragma unroll
      for (int mt = 0; mt < 4; ++mt) a[kl & 1][mt] = lds8(((kl & 1) ? a1 : a0) + (kl >> 1) * 8192 + mt * 2048);
    }
    SBAR();
#pragma unroll
    for (int mt = 0; mt < 4; ++mt)
#pragma unroll
      for (int t = 0; t < 2; ++t)
        acc[t][mt] = VW ? mfma16(a[ks & 1][mt], bw[ks][t], acc[t][mt]) : mfma16(bw[ks][t], a[ks & 1][mt], acc[t][mt]);
    SBAR();
    ldb(ks + Q_AHEAD);
    SBAR();
  }
  STAMP(5);
  const rsrc_t wsr = mk_rsrc(ws, WS_END);
  if (!VW) {
    const float qs = 0.22360679774997896f * 1.4426950408889634f;
    const int s0 = pi * 64 + prow(0, r);
    if (wv < 4) {
      const float sc = wv < 2 ? qs : 1.0f;
      const int voff = (8 * j + 4 * (wv & 1) + g) * (S * 16) + s0 * 16;
#pragma unroll
      for (int mt = 0; mt < 4; ++mt) bst16(wsr, voff, (wv < 2 ? WS_QA : WS_KA) + prow(mt, 0) * 16, pk8(acc[0][mt] * sc, acc[1][mt] * sc));
    } else {
      const int voff = (4 * j + g) * (S * 8) + s0 * 8;
#pragma unroll
      for (int mt = 0; mt < 4; ++mt) {
        bst8(wsr, voff, WS_QB + prow(mt, 0) * 8, pk4(acc[0][mt] * qs));
        bst8(wsr, voff, WS_KB + prow(mt, 0) * 8, pk4(acc[1][mt]));
      }
    }
  } else {
#pragma unroll
    for (int t = 0; t < 2; ++t) {
      if (t == 1 && wv == 7) break;
      const int o = 16 * (2 * wv + t - 10) + r, hh = o / DH, d = o - hh * DH;
      const int voff = (((4 * j + hh) * 256 + pi * 8 + g) * NV + d) * 16;
      bst16(wsr, voff, WS_VT, pk8(acc[t][0], acc[t][1]));
      bst16(wsr, voff, WS_VT + 4 * NV * 16, pk8(acc[t][2], acc[t][3]));
    }
  }
  STAMP(2);
  STAMP_END(ws, 1, 3);
}
__global__ __launch_bounds__(768) void k_qkv(const u16* __restrict__ x, char* __restrict__ ws) {
  extern __shared__ __attribute__((aligned(16))) char smem[];
  const int wv = ufl(TID >> 6), lane = TID & 63;
  const int pi = ((BID & 7) >> 1) * 8 + ((BID >> 3) & 7), j = (BID & 1) * 4 + (BID >> 6);
  const unsigned lds0 = (unsigned)(uintptr_t)smem;
  if (wv >= 9) { qkv_mma<true>(ws, lds0, wv - 4, pi, j); return; }
  if (wv >= 4) { qkv_mma<false>(ws, lds0, wv - 4, pi, j); return; }
  TS_DECL; STAMP(0);
  const int r = lane & 15, g = lane >> 4;
  u32x4 xv[4][5];
  {
    const rsrc_t xr = mk_rsrc(x + (size_t)(pi * 64 + wv * 16) * D, 16 * D * 2);
#pragma unroll
    for (int p = 0; p < 4; ++p)
#pragma unroll
      for (int i = 0; i < 5; ++i) xv[p][i] = bld16(xr, g * (D * 2) + r * 16, p * 4 * D * 2 + i * 256);
  }
#pragma unroll
  for (int i = 0; i < 5; ++i) glds16(ws + WS_MOD + i * 1024, lane * 16, lds0 + Q_GS + wv * 5120 + i * 1024);
  if (wv == 1) {
    const unsigned cv = (lane & 1) ? 0u : 0x3f803f80u;
    bst16(mk_rsrc(ws, WS_END), (((4 * j + g) * 256 + pi * 8 + (r >> 1)) * NV + DH + (lane & 1)) * 16, WS_VT, u32x4{cv, cv, cv, cv});
  }
  SBAR();
  STAMP(6);
  const unsigned gsa = lds0 + Q_GS + wv * 5120 + r * 32;
  f32x4 gs[2][4];
  auto ldgs = [&](int i) {
    gs[i & 1][0] = *(const LAS f32x4*)(gsa + i * 512); gs[i & 1][1] = *(const LAS f32x4*)(gsa + i * 512 + 16);
    gs[i & 1][2] = *(const LAS f32x4*)(gsa + 2560 + i * 512); gs[i & 1][3] = *(const LAS f32x4*)(gsa + 2560 + i * 512 + 16);
  };
  float rs[4], nb[4];
#pragma unroll
  for (int p = 0; p < 4; ++p) {
    float s1 = 0.f, s2 = 0.f;
#pragma unroll
    for (int i = 0; i < 5; ++i)
#pragma unroll
      for (int e = 0; e < 4; ++e) { const unsigned w = xv[p][i][e]; s1 = dot2(w, 0x3f803f80u, s1); s2 = dot2(w, w, s2); }
    const float mu = row16_sum(s1) * (1.0f / D);
    const float var = __builtin_fmaxf(row16_sum(s2) * (1.0f / D) - mu * mu, 0.f);
    rs[p] = __builtin_amdgcn_rsqf(var + 1e-5f);
    nb[p] = -mu * rs[p];
    PIN(rs[p]); PIN(nb[p]);
    if (p == 2) { WAITV(0); ldgs(0); }
  }
  STAMP(7);
  unsigned wa[2];
#pragma unroll
  for (int h = 0; h < 2; ++h) {
    const int ri = 8 * (wv & 1) + 4 * h + g;
    wa[h] = lds0 + (r >> 3) * 8192 + ((wv >> 1) * 32 + ri) * 128 + (((r & 7) ^ (ri & 7)) << 4);
  }
#pragma unroll
  for (int i = 0; i < 5; ++i) {
    if (i < 4) ldgs(i + 1);
#pragma unroll
    for (int p = 0; p < 4; ++p) {
      u32x4 o;
      PIN(xv[p][i]);
#pragma unroll
      for (int e = 0; e < 4; ++e) {
        const unsigned w = xv[p][i][e];
        const f32x4 &sv = gs[i & 1][e >> 1], &gv = gs[i & 1][2 + (e >> 1)];
        o[e] = pk2(__builtin_fmaf(__builtin_fmaf(bf_lo(w), rs[p], nb[p]), gv[2 * (e & 1)], sv[2 * (e & 1)]),
                   __builtin_fmaf(__builtin_fmaf(bf_hi(w), rs[p], nb[p]), gv[2 * (e & 1) + 1], sv[2 * (e & 1) + 1]));
      }
      *(LAS u32x4*)(wa[p >> 1] + i * 16384 + (p & 1) * 2048) = o;
    }
    BAR();
  }
  STAMP_END(ws, 1, 8);
}

template <int NS, int NF, class F>
DEVI void proj_mma(f32x4 (&c)[5], rsrc_t ar, int vt, int hioff, unsigned lds0, unsigned red, int hw, int lane, F&& late) {
  const int mh = hw >> 1, kp = hw & 1, vf = lane * 16 + mh * 2048;
  frag a[NS][2], b[2][5];
  auto lda = [&](int i) {
    if (i < NF) { a[i][0] = bld16c(ar, vf, i * 8192); a[i][1] = bld16c(ar, vf, i * 8192 + 1024); }
    else if (i < NS) { a[i][0] = bld16c(ar, vt, i * 8192); a[i][1] = bld16c(ar, vt, i * 8192 + hioff); }
  };
  auto upto = [](int ks) { return ks < 3 ? 6 + 2 * ks : ks + 9; };
#pragma unroll
  for (int i = 0; i < upto(0); ++i) { lda(i); SBAR(); }
  const unsigned bb = lds0 + kp * 5120 + lane * 16;
  f32x4 acc[2][5] = {};
#pragma unroll
  for (int t = 0; t < 5; ++t) b[0][t] = lds8(bb + t * 1024);
#pragma unroll
  for (int ks = 0; ks < NS; ++ks) {
    if (ks >= NF) {
      PIN(a[ks][0]); PIN(a[ks][1]);
      const frag lo = a[ks][0], hi = a[ks][1];
      a[ks][0] = frag{lo[0], lo[1], hi[0], hi[1]}; a[ks][1] = frag{lo[2], lo[3], hi[2], hi[3]};
    }
#pragma unroll
    for (int t = 0; t < 5; ++t) {
      if (ks + 1 < NS) b[(ks + 1) & 1][t] = lds8(bb + (ks + 1) * 10240 + t * 1024);
      SBAR();
      acc[0][t] = mfma16(b[ks & 1][t], a[ks][0], acc[0][t]); acc[1][t] = mfma16(b[ks & 1][t], a[ks][1], acc[1][t]);
      SBAR();
    }
#pragma unroll
    for (int i = upto(ks); i < upto(ks + 1); ++i) lda(i);
    if (ks == NS - 5) late();
    SBAR();
  }
#pragma unroll
  for (int t = 0; t < 5; ++t) {
    f32x4 lo = acc[0][t], hi = acc[1][t];
    PIN(lo); PIN(hi);
    *(LAS f32x4*)(red + hw * 5120 + t * 1024 + lane * 16) = kp ? lo : hi;
    c[t] = kp ? hi : lo;
  }
  BAR();
#pragma unroll
  for (int t = 0; t < 5; ++t) c[t] += *(const LAS f32x4*)(red + (hw ^ 1) * 5120 + t * 1024 + lane * 16);
}
DEVI void load_gate(u32x4 (&gv)[5], rsrc_t wsr, int g, int off) {
#pragma unroll
  for (int t = 0; t < 4; ++t) gv[t] = bld16(wsr, g * 32, off + (t >> 1) * 128 + (t & 1) * 16);
  gv[4] = bld16(wsr, g * 16, off + 256);
}

template <int NT, bool FIRST>
DEVI void sp_loop(f32x4 (&acc)[2][4], rsrc_t br, int voff, unsigned ab, int rot) {
  constexpr int W = 4;
  frag bw[W][2], a[2][4];
  auto issue = [&](int kb, int i) {
    const int o = kb < 32 ? ((kb + rot) & 31) * 2048 : (1 << 20);
    bw[i][0] = bld16c(br, voff, o);
    if (NT > 1) bw[i][1] = bld16c(br, voff + 65536, o);
  };
#pragma unroll
  for (int i = 0; i < W - (FIRST ? 1 : 0); ++i) issue(i, i);
#pragma unroll
  for (int mt = 0; mt < 4; ++mt) a[0][mt] = lds8(ab + mt * 2048);
#pragma nounroll
  for (int k0 = 0; k0 < 32; k0 += W) {
    const unsigned ac = ab + (k0 & 15) * 8192, an = ab + ((k0 + W) & 15) * 8192;
#pragma unroll
    for (int sl = 0; sl < W; ++sl) {
      if ((sl & 1) == 0) RAWBAR();
#pragma unroll
      for (int mt = 0; mt < 4; ++mt) a[(sl + 1) & 1][mt] = lds8((sl + 1 < W ? ac + (sl + 1) * 8192 : an) + mt * 2048);
      SBAR();
      if (FIRST) { issue(k0 + sl + W - 1, (sl + W - 1) % W); SBAR(); }
#pragma unroll
      for (int t = 0; t < NT; ++t)
#pragma unroll
        for (int mt = 0; mt < 4; ++mt) acc[t][mt] = mfma16(bw[sl][t], a[sl & 1][mt], acc[t][mt]);
      SBAR();
      if (!FIRST) { issue(k0 + sl + W, sl); SBAR(); }
    }
  }
}

constexpr int AT_C = 512;
constexpr int AT_KB = 2 * AT_C * 16, AT_V = AT_KB + AT_C * 8, AT_SLOT = AT_V + (AT_C / 8) * NV * 16;
constexpr int AT_RED = 3 * AT_SLOT, M_PART = AT_RED + 8 * 3072, M_LN = M_PART + 4096, M_UV = M_LN + 512, AT_SCR = M_UV + 1792,
              AT_SYNC = AT_SCR + 256, AT_LDS = 160 * 1024, M_X = 100 * 1024, M_IMG = 48 * 1024,
              G_RED = 128 * 1024, G_OUT = 140 * 1024;
constexpr int CT_U = 0, CT_O = 32, CT_X1 = 40, CT_W = 72, CT_Z = 73;
DEVI float max16(const f32x16& p) {
  float a = __builtin_fmaxf(__builtin_fmaxf(p[0], p[1]), p[2]), b = __builtin_fmaxf(__builtin_fmaxf(p[3], p[4]), p[5]);
  a = __builtin_fmaxf(__builtin_fmaxf(a, p[6]), p[7]); b = __builtin_fmaxf(__builtin_fmaxf(b, p[8]), p[9]);
  a = __builtin_fmaxf(__builtin_fmaxf(a, p[10]), p[11]); b = __builtin_fmaxf(__builtin_fmaxf(b, p[12]), p[13]);
  a = __builtin_fmaxf(__builtin_fmaxf(a, p[14]), p[15]);
  float m = __builtin_fmaxf(a, b);
  auto rr = __builtin_amdgcn_permlane32_swap(__float_as_uint(m), __float_as_uint(m), false, false);
  return __builtin_fmaxf(__uint_as_float(rr[0]), __uint_as_float(rr[1]));
}
DEVI frag pack8(const f32x16& p, int o) {
  return frag{pk2(p[o], p[o + 1]), pk2(p[o + 2], p[o + 3]), pk2(p[o + 4], p[o + 5]), pk2(p[o + 6], p[o + 7])};
}
DEVI int win_col(int n) {
  if (n >= FF) { const int e = n - FF; return (e >> 2) * 112 + 108 + (e & 3); }
  const int seg = n < FH, nn = n - (seg ? 0 : FH), j = nn / 108;
  return (2 * j + seg) * 112 + ncol(nn - j * 108, 3);
}
DEVI void tile_win(const u16* __restrict__ w_in, char* ws, int f, int lane) {
  const int k16 = f / 14, n = (f - k16 * 14) * 128 + 2 * lane;
  typedef __attribute__((address_space(4))) const float cfloat;
  cfloat* mod = (cfloat*)(ws + WS_MOD);
  unsigned r[2][8]; int k0[2], k1[2];
  const rsrc_t wr = mk_rsrc(w_in, D * FF * 2), wsr = mk_rsrc(ws, WS_END);
#pragma unroll
  for (int h = 0; h < 2; ++h) { bool ok; krows<80, 2>(k16 >> 1, 2 * (k16 & 1) + h, k0[h], k1[h], ok); rows8(r[h], wr, n < FF ? n * 2 : D * FF * 2, FF * 2, k0[h], k1[h]); }
  const int n0 = win_col(n), n1 = win_col(n + 1);
  const int db = WS_WIN + (k16 >> 1) * 1024 + (k16 & 1) * 512;
  const int d0 = db + (n0 >> 4) * 20480 + (n0 & 15) * 16, d1 = db + (n1 >> 4) * 20480 + (n1 & 15) * 16;
  float u0 = 0.f, u1 = 0.f, v0 = 0.f, v1 = 0.f;
#pragma unroll
  for (int h = 0; h < 2; ++h) {
    u32x4 c0, c1;
#pragma unroll
    for (int i = 0; i < 8; i += 2) {
      const int ka = (i < 4 ? k0[h] : k1[h] - 4) + i;
      const float ga = mod[4 * D + ka], gb = mod[4 * D + ka + 1], sa = mod[3 * D + ka], sb = mod[3 * D + ka + 1];
      const float a0 = bf_lo(r[h][i]), a1 = bf_hi(r[h][i]), b0 = bf_lo(r[h][i + 1]), b1 = bf_hi(r[h][i + 1]);
      const unsigned w0 = pk2(a0 * ga, b0 * gb), w1 = pk2(a1 * ga, b1 * gb);
      c0[i >> 1] = w0; c1[i >> 1] = w1;
      u0 = dot2(w0, 0x3f803f80u, u0); u1 = dot2(w1, 0x3f803f80u, u1);
      v0 = __builtin_fmaf(a0, sa, __builtin_fmaf(b0, sb, v0)); v1 = __builtin_fmaf(a1, sa, __builtin_fmaf(b1, sb, v1));
    }
    bst16c(wsr, d0, h * 256, c0); bst16c(wsr, d1, h * 256, c1);
  }
  bst16c(wsr, (k16 * 1792 + n) * 8, WS_UVP, __builtin_bit_cast(u32x4, f32x4{u0, v0, u1, v1}));
}
__global__ __launch_bounds__(768) void k_mid(const u16* __restrict__ w_in, const u16* __restrict__ x, const u16* __restrict__ w_sp,
                                             const u16* __restrict__ b_sp, u16* __restrict__ out, char* __restrict__ ws) {
  extern __shared__ __attribute__((aligned(16))) char smem[];
  const int tid = TID, lane = tid & 63, hw = ufl(tid >> 6), wv = hw - 4, r32 = lane & 31, hi = lane >> 5;
  const int head = (BID & 7) * 4 + ((BID >> 3) & 3), qb = BID >> 5;
  const unsigned lds0 = (unsigned)(uintptr_t)smem;
  LAS unsigned* sync = (LAS unsigned*)(lds0 + AT_SYNC);
  const int pi = (BID & 7) * 4 + ((BID >> 3) & 3), j = BID >> 5;
  const int rot = BID >> 3;
  GAS unsigned* ctr = (GAS unsigned*)(ws + WS_CTR);
  LAS f32x2* part = (LAS f32x2*)(lds0 + M_PART);
  LAS f32x2* lnp = (LAS f32x2*)(lds0 + M_LN);
  LAS f32x2* uvp = (LAS f32x2*)(lds0 + M_UV);
  const rsrc_t wsr = mk_rsrc(ws, WS_END);
  const char* KA = ws + WS_KA + head * (S * 32);
  const char* KB = ws + WS_KB + head * (S * 8);
  const char* VT = ws + WS_VT + head * (256 * NV * 16);
  TS_DECL; STAMP(0);
  if (hw < 4) {
    __builtin_amdgcn_s_setprio(2);
    auto stage = [&](int ch) {
      const unsigned sl = lds0 + ((ch + 2) % 3) * AT_SLOT;
#pragma unroll
      for (int i = 0; i < 2; ++i) {
        glds16(KA + ch * (AT_C * 16) + (hw + 4 * i) * 1024, lane * 16, sl + (hw + 4 * i) * 1024);
        glds16(KA + (S + ch * AT_C) * 16 + (hw + 4 * i) * 1024, lane * 16, sl + AT_C * 16 + (hw + 4 * i) * 1024);
      }
      glds16(KB + ch * (AT_C * 8) + hw * 1024, lane * 16, sl + AT_KB + hw * 1024);
#pragma unroll
      for (int i = 0; i < 6; ++i) {
        const int pc = min(hw + 4 * i, NV - 1);
        glds16(VT + ch * (NV * 1024) + pc * 1024, lane * 16, sl + AT_V + pc * 1024);
      }
    };
    auto landed = [&](int ch) { if (lane == 0) __hip_atomic_fetch_add(sync + ch, 1u, __ATOMIC_RELAXED, __HIP_MEMORY_SCOPE_WORKGROUP); };
    stage(0);
    if (tid < 16) sync[tid] = 0u;
    BAR();
    WAITV(0); landed(0);
    stage(1); stage(2);
    WAITV(11); landed(1);
    WAITV(0); landed(2);
    STAMP(6);
    const int f = BID * 4 + hw;
    const unsigned WSCR = lds0 + AT_SCR;
    if (f < 560) { tile_win(w_in, ws, f, lane); signal_done(sync + 7, 4u, ctr + CT_W * 32, lane); }
    STAMP(5);
    warm((const char*)w_sp + ((BID & 7) * 128 + (BID >> 3) * 4 + hw) * 8192, 1, lane, WSCR);
    wait_lds<8>(sync + 4, 8u);
    stage(3);
    WAITV(0); landed(3);
    wait_global(ctr + CT_W * 32, 140u);
    {
      const int o = 56 * (hw & 1) + lane, seg = hw >> 1;
      const int vo = (lane < 56 && o < 108) ? ((seg ? 0 : FH) + 108 * j + o) * 8 : WS_END;
      float u = 0.f, v = 0.f;
#pragma unroll
      for (int h = 0; h < 2; ++h) {
        u32x2 t[20];
#pragma unroll
        for (int i = 0; i < 20; ++i) t[i] = bld8c(wsr, vo, WS_UVP + (h * 20 + i) * 1792 * 8);
#pragma unroll
        for (int i = 0; i < 20; ++i) { u += __uint_as_float(t[i][0]); v += __uint_as_float(t[i][1]); }
      }
      if (lane < 56) uvp[seg * 112 + ncol(o, 3)] = f32x2{u, v};
    }
    {
      const int i = (BID >> 3) * 4 + hw;
      warm(ws + WS_WIN + i * 8192, 1, lane, WSCR); warm(ws + WS_WIN + (128 + i) * 8192, 1, lane, WSCR);
      if (i < 24) warm(ws + WS_WIN + (256 + i) * 8192, 1, lane, WSCR);
    }
    const char* wsrc = ws + WS_WO + (j * 100 + hw) * 1024;
    auto issue_w = [&](int n0, int n1) {
#pragma unroll
      for (int n = n0; n < n1; ++n) glds16(wsrc + n * 4096, lane * 16, lds0 + hw * 1024 + n * 4096);
    };
    wait_lds<8>(sync + 5, 8u); issue_w(0, 10);
    wait_lds<8>(sync + 6, 8u); issue_w(10, 21);
    STAMP(7);
    BAR();
    wait_lds<1>(sync + 8, 8u);
    issue_w(21, 25);
#pragma unroll
    for (int i = 0; i < 3; ++i) {
      const int pc = min(hw + 4 * i, 9), ch = pc * 64 + lane, row = ch / 10;
      glds16(x + (size_t)pi * 64 * D + 80 * j, prow(row >> 4, row & 15) * (D * 2) + (ch - row * 10) * 16, lds0 + M_X + pc * 1024);
    }
    WAITV(0);
    RAWBAR();
    STAMP(8);
    const int r = lane & 15, g = lane >> 4;
    const int xo = WS_X1 + pi * 81920;
    u32x4 y0, y1; u32x2 yt;
    {
      u32x4 gv[5]; f32x4 c[5];
      proj_mma<10, 8>(c, mk_rsrc(ws + WS_O + pi * 81920 + (hw & 1) * 4096, 81920), g * 1024 + (hw >> 1) * 256 + r * 16, 512, lds0, lds0 + AT_RED, hw, lane,
                      [&]() { load_gate(gv, wsr, g, WS_MOD + 2 * D * 4 + 80 * j * 4); });
      const unsigned xa = lds0 + M_X + (hw * 16 + r) * 160;
      const u32x4 r0 = lds8(xa + g * 16), r1 = lds8(xa + 64 + g * 16); const u32x2 rt = lds4(xa + 128 + g * 8);
      y0 = gated(r0, gv[0], gv[1], c[0], c[1]); y1 = gated(r1, gv[2], gv[3], c[2], c[3]);
      yt = pk4(up4(rt[0], rt[1]) + __builtin_bit_cast(f32x4, gv[4]) * c[4]);
      bst16c(wsr, lane * 16, xo + 2 * j * 4096 + hw * 1024, y0);
      bst16c(wsr, lane * 16, xo + (2 * j + 1) * 4096 + hw * 1024, y1);
      bst8c(wsr, lane * 8, xo + 65536 + j * 2048 + hw * 512, yt);
      STAMP(9);
      signal_done(sync + 9, 4u, ctr + (CT_X1 + pi) * 32, lane);
    }
    RAWBAR();
    STAMP(10);
    {
      frag a[16]; u32x2 at[8];
      float s1 = 0.f, s2 = 0.f;
      auto ldx = [&](int gq) {
#pragma unroll
        for (int i = 0; i < 4; ++i) {
          if (gq < 4) a[4 * gq + i] = bld16c(wsr, lane * 16, xo + (4 * gq + i) * 4096 + hw * 1024);
          else { at[2 * i] = bld8c(wsr, lane * 8, xo + 65536 + i * 4096 + hw * 512); at[2 * i + 1] = bld8c(wsr, lane * 8, xo + 65536 + i * 4096 + 2048 + hw * 512); }
          SBAR();
        }
      };
      auto fr = [&](int i) { return i < 16 ? a[i] : frag{at[2 * i - 32][0], at[2 * i - 32][1], at[2 * i - 31][0], at[2 * i - 31][1]}; };
      ldx(0); ldx(1); ldx(2);
#pragma unroll
      for (int gq = 0; gq < 6; ++gq) {
        if (gq < 5) {
#pragma unroll
          for (int i = 4 * gq; i < 4 * gq + 4; ++i) *(LAS frag*)(lds0 + M_IMG + i * 4096 + hw * 1024 + lane * 16) = fr(i);
        }
        SBAR();
        if (gq > 0) {
#pragma unroll
          for (int i = 4 * gq - 4; i < 4 * gq; ++i) {
            const frag w = fr(i);
#pragma unroll
            for (int e = 0; e < 4; ++e) { const unsigned v = w[e]; s1 = dot2(v, 0x3f803f80u, s1); s2 = dot2(v, v, s2); }
          }
          PIN(s1); PIN(s2);
        }
        if (gq < 5) BAR();
        SBAR();
        if (gq == 0) { ldx(3); ldx(4); }
      }
      s1 += __shfl_xor(s1, 16); s2 += __shfl_xor(s2, 16); s1 += __shfl_xor(s1, 32); s2 += __shfl_xor(s2, 32);
      const float mu = s1 * (1.0f / D);
      const float rs = __builtin_amdgcn_rsqf(__builtin_fmaxf(s2 * (1.0f / D) - mu * mu, 0.f) + 1e-5f);
      if (g == 0) lnp[hw * 16 + r] = f32x2{rs, -mu * rs};
      BAR();
    }
    STAMP_END(ws, 2, 4);
    {
      TS_DECL; STAMP(0);
      const int rr = lane >> 3;
      const char* src = (const char*)w_sp + (size_t)(pi * 64 + prow(hw, 0)) * (S * 2);
      const unsigned sv0 = prow(0, rr) * (S * 2) + (((lane & 7) ^ rr) << 4), dst = lds0 + hw * 2048;
      auto issue = [&](int kb) {
        const char* sp = src + ((kb + rot) & 31) * 128;
        const unsigned d = dst + (kb & 15) * 8192;
        glds16(sp, sv0, d); glds16(sp + 16 * (S * 2), sv0, d + 1024);
      };
      const char* osrc = ws + WS_WOUT + (j * 140 + hw) * 1024;
      auto issue_o = [&](int n0, int n1) {
#pragma unroll
        for (int n = n0; n < n1; ++n) glds16(osrc + n * 4096, lane * 16, lds0 + hw * 1024 + n * 4096);
      };
#pragma nounroll
      for (int kb = 0; kb < 6; ++kb) issue(kb);
      RAWBAR();
      wait_lds<1>(sync + 11, 7u);
#pragma nounroll
      for (int kb = 6; kb < 16; ++kb) issue(kb);
      WAITV(0);
      RAWBAR();
      RAWBAR();
#pragma nounroll
      for (int m = 1; m < 9; ++m) { WAITV(8); RAWBAR(); issue(2 * m + 14); issue(2 * m + 15); }
      WAITV(4); RAWBAR(); WAITV(0); RAWBAR(); RAWBAR(); RAWBAR(); RAWBAR();
      RAWBAR(); issue_o(0, 8);
      RAWBAR(); issue_o(8, 16);
      STAMP(9);
      BAR();
      wait_lds<1>(sync + 12, 8u);
      issue_o(16, 35);
      STAMP(4);
      WAITV(0);
      RAWBAR();
      STAMP(6);
      u32x4 gv[5]; f32x4 c[5];
      proj_mma<14, 12>(c, mk_rsrc(ws + WS_U + pi * (28 * 4096) + (hw & 1) * 4096, 28 * 4096), lane * 16 + (hw >> 1) * 1024, 2048, lds0, lds0 + G_OUT, hw, lane,
                       [&]() { load_gate(gv, wsr, g, WS_MOD + 5 * D * 4 + 80 * j * 4); });
      STAMP(8);
      const unsigned st = lds0 + G_OUT + (hw ^ 1) * 5120, xa = st + r * 160;
      *(LAS u32x4*)(xa + g * 16) = gated(y0, gv[0], gv[1], c[0], c[1]);
      *(LAS u32x4*)(xa + 64 + g * 16) = gated(y1, gv[2], gv[3], c[2], c[3]);
      *(LAS u32x2*)(xa + 128 + g * 8) = pk4(up4(yt[0], yt[1]) + __builtin_bit_cast(f32x4, gv[4]) * c[4]);
      const rsrc_t orr = mk_rsrc(out + (size_t)pi * 64 * D + 80 * j, 64 * D * 2 - 160 * j);
#pragma unroll
      for (int i = 0; i < 3; ++i) {
        const int ch = i * 64 + lane, row = ch / 10;
        if (ch < 160) bst16(orr, prow(hw, row) * (D * 2) + (ch - row * 10) * 16, 0, lds8(st + ch * 16));
      }
      STAMP_END(ws, 5, 11);
    }
    return;
  }
  const int qg = wv & 3, kh = wv >> 2;
  const int q0 = qb * 256 + qg * 64 + r32;
  const frag qaA = bld16(wsr, ((head * 2 + hi) * S + q0) * 16, WS_QA), qaB = bld16(wsr, ((head * 2 + hi) * S + q0) * 16, WS_QA + 512);
  u32x2 qbA = bld8(wsr, (head * S + q0) * 8, WS_QB), qbB = bld8(wsr, (head * S + q0) * 8, WS_QB + 256);
  RAWBAR();
  const int pk = (r32 & 0x13) | ((r32 & 4) << 1) | ((r32 & 8) >> 1);
  const unsigned s0 = lds0 + 2 * AT_SLOT, kz = s0 + hi * (AT_C * 16) + pk * 16, bz = s0 + AT_KB + pk * 8;
  const unsigned ka0 = hi * (AT_C * 16) + pk * 16 + kh * 4096, kb0 = AT_KB + pk * 8 + kh * 2048;
  const unsigned v0 = AT_V + (hi * NV + min(r32, NV - 1)) * 16 + kh * (32 * NV * 16);
  if (hi) { qbA = u32x2{0u, 0u}; qbB = u32x2{0u, 0u}; }
  PIN(qbA); PIN(qbB);
  wait_lds<1>(sync, 4u);
  STAMP(1);
  f32x16 oA = {}, oB = {}, nmA, nmB, sA, sB;
  frag ka, va, vb, plo, phi = {0u, 0u, 0u, 0u}; u32x2 kb;
  {
    ka = lds8(kz); kb = lds4(bz);
    sA = mfma32(ka, qaA, f32x16{}); sB = mfma32(ka, qaB, f32x16{});
    sA = mfma32k8(kb, qbA, sA); sB = mfma32k8(kb, qbB, sB);
    ka = lds8(s0 + ka0); kb = lds4(s0 + kb0); vb = lds8(s0 + v0); va = lds8(s0 + v0);
    const float mA = max16(sA), mB = max16(sB);
#pragma unroll
    for (int r = 0; r < 16; ++r) { nmA[r] = -mA; nmB[r] = -mB; }
    PIN(nmA); PIN(nmB);
    sA = mfma32(ka, qaA, nmA); sA = mfma32k8(kb, qbA, sA);
  }
#define PMASK 0x1111
#define PRIO(i) do { if ((((PMASK >> (i)) ^ (PMASK >> (((i) + 15) & 15))) & 1) != 0) { \
    asm volatile("s_cmp_eq_u32 %0, 0\n\ts_cbranch_scc1 1\n\ts_setprio %1" :: "s"(kh), "i"(((PMASK >> (i)) & 1) ? 0 : 1) : "scc"); SBAR(); } } while (0)
#define EX4(C, i, P, w) do { C[i] = __builtin_amdgcn_exp2f(C[i]); C[i + 1] = __builtin_amdgcn_exp2f(C[i + 1]); C[i + 2] = __builtin_amdgcn_exp2f(C[i + 2]); \
    C[i + 3] = __builtin_amdgcn_exp2f(C[i + 3]); P[w] = pk2(C[i], C[i + 1]); P[w + 1] = pk2(C[i + 2], C[i + 3]); PIN(P); SBAR(); } while (0)
  for (int ch = 0; ch < 4; ++ch) {
    const unsigned cur = lds0 + ((ch + 2) % 3) * AT_SLOT, nxt = lds0 + (ch % 3) * AT_SLOT;
    const unsigned kc = cur + ka0, kn = nxt + ka0, bc = cur + kb0, bn = nxt + kb0, vc = cur + v0, vn = nxt + v0;
#pragma unroll
    for (int t = 0; t < 8; ++t) {
      if (t == 7 && ch < 3) wait_lds<4>(sync + ch + 1, 4u);
      if (t == 1 && ch >= 1 && lane == 0) __hip_atomic_fetch_add(sync + 3 + ch, 1u, __ATOMIC_RELAXED, __HIP_MEMORY_SCOPE_WORKGROUP);
      PRIO(2 * t);
      sB = mfma32(ka, qaB, nmB); SBAR(); ka = lds8(t < 7 ? kc + (t + 1) * 512 : kn); SBAR(); EX4(sA, 0, plo, 0);
      oB = mfma32(vb, phi, oB); SBAR(); vb = lds8(vc + (4 * t + 2) * (NV * 16)); SBAR(); EX4(sA, 4, plo, 2);
      sB = mfma32k8(kb, qbB, sB); SBAR(); kb = lds4(t < 7 ? bc + (t + 1) * 256 : bn); SBAR(); EX4(sA, 8, phi, 0);
      oA = mfma32(va, plo, oA); SBAR(); EX4(sA, 12, phi, 2);
      PRIO(2 * t + 1);
      sA = mfma32(ka, qaA, nmA); SBAR(); EX4(sB, 0, plo, 0);
      oA = mfma32(vb, phi, oA); SBAR(); EX4(sB, 4, plo, 2);
      sA = mfma32k8(kb, qbA, sA); SBAR(); EX4(sB, 8, phi, 0);
      oB = mfma32(va, plo, oB); SBAR(); va = lds8(t < 7 ? vc + (4 * t + 4) * (NV * 16) : vn); SBAR(); EX4(sB, 12, phi, 2);
    }
  }
  oB = mfma32(vb, phi, oB);
#undef EX4
  STAMP(2);
  f32x16 o;
  {
    PIN(oA); PIN(oB);
    const unsigned rw = lds0 + AT_RED + wv * 3072 + lane * 16, rd = lds0 + AT_RED + (wv ^ 4) * 3072 + lane * 16;
#pragma unroll
    for (int i = 0; i < 3; ++i) {
      f32x4 w;
#pragma unroll
      for (int e = 0; e < 4; ++e) { w[e] = kh ? oA[4 * i + e] : oB[4 * i + e]; o[4 * i + e] = kh ? oB[4 * i + e] : oA[4 * i + e]; }
      *(LAS f32x4*)(rw + i * 1024) = w;
    }
    BAR();
#pragma unroll
    for (int i = 0; i < 3; ++i) {
      const f32x4 w = *(const LAS f32x4*)(rd + i * 1024);
#pragma unroll
      for (int e = 0; e < 4; ++e) o[4 * i + e] += w[e];
    }
  }
  float l = __shfl(o[8], r32 + 32);
  float chk = 0.f;
#pragma unroll
  for (int r = 0; r < 12; ++r) chk += __builtin_fabsf(o[r]);
  if (__builtin_expect(__any(!(chk < 3.0e38f)), 0)) {
    const frag qa = kh ? qaB : qaA; const u32x2 qb4 = kh ? qbB : qbA;
    o = f32x16{};
    float m = -INFINITY;
    for (int t = 0; t < 64; ++t) {
      const frag k1 = *(const frag*)(KA + (hi * S + t * 32 + pk) * 16);
      const u32x2 k2 = *(const u32x2*)(KB + (t * 32 + pk) * 8);
      const char* vp = VT + ((t * 4 + hi) * NV + min(r32, NV - 1)) * 16;
      const frag v1 = *(const frag*)vp, v2 = *(const frag*)(vp + 2 * NV * 16);
      f32x16 s = mfma32(k1, qa, f32x16{}); s = mfma32k8(k2, qb4, s);
      const float mn = __builtin_fmaxf(m, max16(s));
      const float al = __builtin_amdgcn_exp2f(m - mn);
      m = mn;
#pragma unroll
      for (int r = 0; r < 16; ++r) { s[r] = __builtin_amdgcn_exp2f(s[r] - mn); o[r] *= al; }
      o = mfma32(v1, pack8(s, 0), o); o = mfma32(v2, pack8(s, 8), o);
    }
    l = __shfl(o[8], r32 + 32);
  }
  const float rl = 1.0f / l;
  const int pos = pinv(32 * kh + r32);
  const int vo = (qb * 4 + qg) * (80 * 1024) + (pos >> 4) * 1024 + (pos & 15) * 16;
  bst16c(wsr, vo + hi * 256, WS_O + (head >> 1) * 4096 + (head & 1) * 512,
         u32x4{pk2(o[0] * rl, o[1] * rl), pk2(o[2] * rl, o[3] * rl), pk2(o[4] * rl, o[5] * rl), pk2(o[6] * rl, o[7] * rl)});
  if (!hi) bst8c(wsr, (qb * 4 + qg) * 81920 + kh * 256 + (pos & 15) * 16 + ((pos >> 4) & 1) * 8, WS_O + 65536 + head * 512,
                 u32x2{pk2(o[8] * rl, o[9] * rl), pk2(o[10] * rl, o[11] * rl)});
  __builtin_amdgcn_s_setprio(3);
  signal_done(sync + 8, 8u, ctr + (CT_O + qb) * 32, lane);
  STAMP(3);
  GAS unsigned long long* gran = (GAS unsigned long long*)(ws + WS_STAT) + pi * 512;
  const int r = lane & 15, g = lane >> 4;
  if (wv == 7) {
    wait_global(ctr + (CT_O + (pi >> 2)) * 32, 32u);
    RAWBAR(); RAWBAR();
    wait_global(ctr + (CT_X1 + pi) * 32, 8u);
#pragma unroll
    for (int i = 0; i < 7; ++i) RAWBAR();
    wait_lds<1>(sync + 10, 7u);
    float t1 = 0.f, t2 = 0.f;
#pragma unroll
    for (int w = 0; w < 7; ++w) { f32x2 p = part[w * 64 + lane]; t1 += p[0]; t2 += p[1]; }
    unsigned b1w = __float_as_uint(t1), b2w = __float_as_uint(t2);
    if (b2w == ~0u) b2w = 0x7fc00000u;
    __hip_atomic_store(gran + j * 64 + lane, ((unsigned long long)b2w << 32) | b1w, __ATOMIC_RELAXED, __HIP_MEMORY_SCOPE_AGENT);
    for (unsigned spins = 0;; ++spins) {
      bool ok = true; t1 = 0.f; t2 = 0.f;
#pragma unroll
      for (int w = 0; w < 8; ++w) {
        unsigned long long v = __hip_atomic_load(gran + w * 64 + lane, __ATOMIC_RELAXED, __HIP_MEMORY_SCOPE_AGENT);
        ok &= (unsigned)(v >> 32) != ~0u;
        t1 += __uint_as_float((unsigned)v); t2 += __uint_as_float((unsigned)(v >> 32));
      }
      if (__all(ok) || spins > (1u << 20)) break;
      __builtin_amdgcn_s_sleep(1);
    }
    const float mu = t1 * (1.0f / FH);
    const float rs = __builtin_amdgcn_rsqf(__builtin_fmaxf(t2 * (1.0f / FH) - mu * mu, 0.f) + 1e-5f);
    part[7 * 64 + lane] = f32x2{rs, -mu * rs};
    STAMP(14);
    BAR();
    wait_global(ctr + (CT_Z + j) * 32, 32u);
  } else {
  __builtin_amdgcn_s_setprio(0);
  const rsrc_t wr = mk_rsrc(ws + WS_WIN + (j * 14 + wv) * 20 * 1024, 8 * 20 * 1024);
  frag bw[40], a[3][4];
  auto ldb = [&](int i) { if (i < 40) bw[i] = bld16c(wr, lane * 16, (i < 20 ? i : 120 + i) * 1024); };
  constexpr int WB = 18;
#pragma unroll
  for (int i = 0; i < 8; ++i) ldb(i);
  RAWBAR(); RAWBAR();
#pragma unroll
  for (int i = 8; i < WB; ++i) ldb(i);
  RAWBAR();
  f32x4 z2[4] = {}, z1[4] = {};
  const unsigned ab = lds0 + M_IMG + lane * 16;
#pragma unroll
  for (int ks = -2; ks < 40; ++ks) {
    const int kl = ks + 2;
    if (kl < 40) {
      if (kl < 20 && (kl & 3) == 0) RAWBAR();
#pragma unroll
      for (int mt = 0; mt < 4; ++mt) a[kl % 3][mt] = lds8(ab + (kl % 20) * 4096 + mt * 1024);
    }
    SBAR();
    if (ks < 0) continue;
#pragma unroll
    for (int mt = 0; mt < 4; ++mt) {
      if (ks < 20) z2[mt] = mfma16(a[ks % 3][mt], bw[ks], z2[mt]);
      else z1[mt] = mfma16(bw[ks], a[ks % 3][mt], z1[mt]);
    }
    SBAR();
    ldb(ks + WB);
    SBAR();
    if (ks == 19) {
      STAMP(11);
      RAWBAR();
      const f32x2 uv = uvp[wv * 16 + r];
      f32x2 st[4];
#pragma unroll
      for (int mt = 0; mt < 4; ++mt) {
        const f32x4 l0 = *(const LAS f32x4*)&lnp[mt * 16 + 4 * g], l1 = *(const LAS f32x4*)&lnp[mt * 16 + 4 * g + 2];
        f32x4 z = {gelu_tanh(__builtin_fmaf(z2[mt][0], l0[0], __builtin_fmaf(l0[1], uv[0], uv[1]))),
                   gelu_tanh(__builtin_fmaf(z2[mt][1], l0[2], __builtin_fmaf(l0[3], uv[0], uv[1]))),
                   gelu_tanh(__builtin_fmaf(z2[mt][2], l1[0], __builtin_fmaf(l1[1], uv[0], uv[1]))),
                   gelu_tanh(__builtin_fmaf(z2[mt][3], l1[2], __builtin_fmaf(l1[3], uv[0], uv[1])))};
        const u32x2 zb = pk4(z);
        z = up4(zb[0], zb[1]);
        z2[mt] = z;
        st[mt] = f32x2{quad4_sum(z), quad4_sum(z * z)};
      }
      if ((r & 3) == 0) {
#pragma unroll
        for (int mt = 0; mt < 4; ++mt) part[wv * 64 + mt * 16 + 4 * g + (r >> 2)] = st[mt];
      }
      asm volatile("s_waitcnt lgkmcnt(0)" ::: "memory");
      if (lane == 0) __hip_atomic_fetch_add(sync + 10, 1u, __ATOMIC_RELAXED, __HIP_MEMORY_SCOPE_WORKGROUP);
      STAMP(12);
    }
  }
  STAMP(13);
  {
    const f32x4 ua = *(const LAS f32x4*)&uvp[112 + wv * 16 + 4 * g], ub = *(const LAS f32x4*)&uvp[112 + wv * 16 + 4 * g + 2];
#pragma unroll
    for (int mt = 0; mt < 4; ++mt) {
      const f32x2 ln = lnp[mt * 16 + r];
      z1[mt][0] = gelu_tanh(__builtin_fmaf(z1[mt][0], ln[0], __builtin_fmaf(ln[1], ua[0], ua[1])));
      z1[mt][1] = gelu_tanh(__builtin_fmaf(z1[mt][1], ln[0], __builtin_fmaf(ln[1], ua[2], ua[3])));
      z1[mt][2] = gelu_tanh(__builtin_fmaf(z1[mt][2], ln[0], __builtin_fmaf(ln[1], ub[0], ub[1])));
      z1[mt][3] = gelu_tanh(__builtin_fmaf(z1[mt][3], ln[0], __builtin_fmaf(ln[1], ub[2], ub[3])));
    }
    *(LAS u32x4*)(lds0 + G_RED + wv * 2048 + lane * 16) = pk8(z1[0], z1[1]);
    *(LAS u32x4*)(lds0 + G_RED + wv * 2048 + 1024 + lane * 16) = pk8(z1[2], z1[3]);
  }
  BAR();
#pragma unroll
  for (int mt = 0; mt < 4; ++mt)
#pragma unroll
    for (int e = 0; e < 4; e += 2) {
      const f32x4 p = *(const LAS f32x4*)&part[7 * 64 + mt * 16 + 4 * g + e];
      z2[mt][e] = __builtin_fmaf(z2[mt][e], p[0], p[1]); z2[mt][e + 1] = __builtin_fmaf(z2[mt][e + 1], p[2], p[3]);
    }
  const int zo = ((j * 7 + wv) * 64 + pi * 2) * 1024 + lane * 16;
  bst16c(wsr, zo, WS_ZT, pk8(z2[0], z2[1])); bst16c(wsr, zo, WS_ZT + 1024, pk8(z2[2], z2[3]));
  __builtin_amdgcn_s_setprio(3);
  signal_done(sync + 11, 7u, ctr + (CT_Z + j) * 32, lane);
  }
  STAMP_END(ws, 2, 15);
  {
    TS_DECL;
    __builtin_amdgcn_s_setprio(0);
    const int p = wv >> 1, kp = wv & 1;
    u32x4 z1v[2]; unsigned bsv[2];
#pragma unroll
    for (int i = 0; i < 2; ++i) {
      bsv[i] = b_sp[pi * 64 + prow(2 * kp + i, r)];
      z1v[i] = lds8(lds0 + G_RED + min(2 * p + i, 6) * 2048 + kp * 1024 + lane * 16);
    }
    const rsrc_t br = mk_rsrc(ws + WS_ZT + (j * 7 + 2 * p) * 65536, 2 * 65536);
    const int voff = lane * 16 + kp * 1024;
    const unsigned ab = afrag_base(lds0, lane, kp);
    f32x4 acc[2][4] = {};
    BAR();
    STAMP(7);
    if (p < 2) sp_loop<2, true>(acc, br, voff, ab, rot);
    else if (p == 2) sp_loop<2, false>(acc, br, voff, ab, rot);
    else sp_loop<1, false>(acc, br, voff, ab, rot);
    STAMP(10);
    __builtin_amdgcn_s_setprio(3);
    const unsigned rw = lds0 + G_RED + (wv < 7 ? wv * 4096 : 26624) + lane * 16, rd = lds0 + G_RED + ((wv ^ 1) < 7 ? (wv ^ 1) * 4096 : 26624) + lane * 16;
    f32x4 c[2][2];
#pragma unroll
    for (int t = 0; t < 2; ++t)
#pragma unroll
      for (int i = 0; i < 2; ++i) {
        f32x4 lo = acc[t][i], hi = acc[t][2 + i];
        PIN(lo); PIN(hi);
        if (t == 0 || p < 3) *(LAS f32x4*)(rw + (t * 2 + i) * 1024) = kp ? lo : hi;
        c[t][i] = kp ? hi : lo;
      }
    BAR();
#pragma unroll
    for (int t = 0; t < 2; ++t)
#pragma unroll
      for (int i = 0; i < 2; ++i) c[t][i] += *(const LAS f32x4*)(rd + (t * 2 + i) * 1024);
    STAMP(2);
    f32x4 y[2][2];
#pragma unroll
    for (int i = 0; i < 2; ++i) {
      const float bs = bf_lo(bsv[i]);
      y[0][i] = up4(z1v[0][2 * i], z1v[0][2 * i + 1]) * (c[0][i] + bs); y[1][i] = up4(z1v[1][2 * i], z1v[1][2 * i + 1]) * (c[1][i] + bs);
    }
    if (p < 3) {
      bst16c(wsr, (pi * 28 + 3 * j + p) * 4096 + kp * 2048 + lane * 16, WS_U, pkI(y[0][0], y[1][0]));
      bst16c(wsr, (pi * 28 + 3 * j + p) * 4096 + kp * 2048 + lane * 16, WS_U + 1024, pkI(y[0][1], y[1][1]));
    } else {
      bst16c(wsr, (pi * 28 + 24) * 4096 + (2 * j + kp) * 1024 + lane * 16, WS_U, pk8(y[0][0], y[0][1]));
    }
    signal_done(sync + 12, 8u, ctr + (CT_U + pi) * 32, lane);
    STAMP(1);
    if (wv == 0) wait_global(ctr + (CT_U + pi) * 32, 8u);
    STAMP_END(ws, 5, 3);
  }
}

#if defined(__HIP_DEVICE_COMPILE__)
#pragma clang attribute pop
#endif
extern "C" void kernel_launch(void* const* d_in, const int* in_sizes, int n_in, void* d_out, int out_size, void* d_ws,
                              size_t ws_size, hipStream_t stream) {
  const u16* x = (const u16*)d_in[0];
  const u16* wqkv = (const u16*)d_in[1];
  const u16* wo = (const u16*)d_in[2];
  const u16* w_in = (const u16*)d_in[4];
  const u16* w_sp = (const u16*)d_in[5];
  const u16* b_sp = (const u16*)d_in[6];
  const u16* w_out = (const u16*)d_in[7];
  const u16* c = (const u16*)d_in[8];
  const u16* w_mod = (const u16*)d_in[9];
  char* ws = (char*)d_ws;
  (void)hipFuncSetAttribute((const void*)k_qkv, hipFuncAttributeMaxDynamicSharedMemorySize, Q_LDS);
  (void)hipFuncSetAttribute((const void*)k_mid, hipFuncAttributeMaxDynamicSharedMemorySize, AT_LDS);
#define REP
  REP k_prep<<<256 + PT_BLOCKS, 256, 0, stream>>>(c, w_mod, wqkv, wo, w_out, x, ws);
  REP k_qkv<<<256, 768, Q_LDS, stream>>>(x, ws);
  REP k_mid<<<256, 768, AT_LDS, stream>>>(w_in, x, w_sp, b_sp, (u16*)d_out, ws);
}
```

```cpp
#include <hip/hip_runtime.h>
#include <hip/hip_bf16.h>
#include <stdint.h>

extern "C" __device__ unsigned long long dispatch_id_(void) __asm("llvm.amdgcn.dispatch.id");
typedef unsigned short u16;
typedef unsigned long long u64;
typedef __attribute__((ext_vector_type(8))) __bf16 bf16v8;
typedef __attribute__((ext_vector_type(2))) __bf16 bf16x2;
typedef __attribute__((ext_vector_type(4))) short s16x4;
typedef __attribute__((ext_vector_type(2))) float f32x2;
typedef __attribute__((ext_vector_type(4))) float f32x4;
typedef __attribute__((ext_vector_type(16))) float f32x16;
typedef __attribute__((ext_vector_type(4))) unsigned u32x4;
typedef __attribute__((ext_vector_type(2))) unsigned u32x2;
typedef u32x4 frag;
#define LAS __attribute__((address_space(3)))
#define GAS __attribute__((address_space(1)))
#define DEVI __device__ __forceinline__
#define TID ((int)__builtin_amdgcn_workitem_id_x())
#define BID ((int)__builtin_amdgcn_workgroup_id_x())

constexpr int S = 2048, D = 640, NH = 32, DH = 20, FF = 1728, FH = 864;
constexpr int NV = 22;

constexpr int WS_MOD  = 0;
constexpr int WS_FLAG = 131072;
constexpr int WS_STAT = WS_FLAG + 128 * 4096;
constexpr int WS_XST  = WS_STAT + 32 * 8 * 64 * 8;
constexpr int WS_UVP  = WS_XST + 32 * 8 * 64 * 8;
constexpr int WS_WQKV = WS_UVP + 40 * 1792 * 8;
constexpr int WS_WO   = WS_WQKV + 8 * 15 * 20 * 1024;
constexpr int WS_WIN  = WS_WO + 40 * 20 * 1024;
constexpr int WS_WOUT = WS_WIN + 8 * 14 * 20 * 1024;
constexpr int WS_QA   = WS_WOUT + 40 * 28 * 1024;
constexpr int WS_QB   = WS_QA + NH * S * 32;
constexpr int WS_KA   = WS_QB + NH * S * 8;
constexpr int WS_KB   = WS_KA + NH * S * 32;
constexpr int WS_VT   = WS_KB + NH * S * 8;
constexpr int WS_O    = WS_VT + NH * 256 * NV * 16;
constexpr int WS_X1   = WS_O + S * D * 2;
constexpr int WS_ZT   = WS_X1 + S * D * 2;
constexpr int WS_U    = WS_ZT + 8 * 7 * 64 * 1024;
constexpr int WS_END  = WS_U + 32 * 28 * 4 * 1024;

constexpr int FSTR = 16;
constexpr int FNAP = 2, PNAP = 12;
constexpr int PD0 = 32, PD1 = 48, DQ = 12, DO = 8, DX = 12, DE = 12, DU = 20, DZ = 6;
constexpr int LNP = 2, PMASK = 0x1111;
constexpr int ZSPLIT = 36, ZDRAIN = 3;

DEVI float bf_lo(unsigned w) { return __uint_as_float(w << 16); }
DEVI float bf_hi(unsigned w) { return __uint_as_float(w & 0xffff0000u); }
DEVI unsigned pk2(float a, float b) { bf16x2 v = {(__bf16)a, (__bf16)b}; return __builtin_bit_cast(unsigned, v); }
DEVI float rbf(float a) { return bf_lo(pk2(a, a)); }
DEVI u32x2 pk4(const f32x4& a) { return u32x2{pk2(a[0], a[1]), pk2(a[2], a[3])}; }
DEVI u32x4 pk8(const f32x4& a, const f32x4& b) { return u32x4{pk2(a[0], a[1]), pk2(a[2], a[3]), pk2(b[0], b[1]), pk2(b[2], b[3])}; }
DEVI u32x4 pkI(const f32x4& a, const f32x4& b) { return u32x4{pk2(a[0], b[0]), pk2(a[1], b[1]), pk2(a[2], b[2]), pk2(a[3], b[3])}; }
DEVI f32x4 upE(const u32x4& w) { return f32x4{bf_lo(w[0]), bf_lo(w[1]), bf_lo(w[2]), bf_lo(w[3])}; }
DEVI f32x4 upO(const u32x4& w) { return f32x4{bf_hi(w[0]), bf_hi(w[1]), bf_hi(w[2]), bf_hi(w[3])}; }
DEVI f32x4 up4(unsigned a, unsigned b) { return f32x4{bf_lo(a), bf_hi(a), bf_lo(b), bf_hi(b)}; }
DEVI float dot2(unsigned a, unsigned b, float c) { return __builtin_amdgcn_fdot2_f32_bf16(__builtin_bit_cast(bf16x2, a), __builtin_bit_cast(bf16x2, b), c, false); }
DEVI f32x4 mfma16(frag a, frag b, f32x4 c) { return __builtin_amdgcn_mfma_f32_16x16x32_bf16(__builtin_bit_cast(bf16v8, a), __builtin_bit_cast(bf16v8, b), c, 0, 0, 0); }
DEVI f32x16 mfma32(frag a, frag b, f32x16 c) { return __builtin_amdgcn_mfma_f32_32x32x16_bf16(__builtin_bit_cast(bf16v8, a), __builtin_bit_cast(bf16v8, b), c, 0, 0, 0); }
DEVI f32x16 mfma32k8(u32x2 a, u32x2 b, f32x16 c) { return __builtin_amdgcn_mfma_f32_32x32x8bf16_1k(__builtin_bit_cast(s16x4, a), __builtin_bit_cast(s16x4, b), c, 0, 0, 0); }
DEVI frag lds8(unsigned a) { return *(const LAS frag*)a; }
DEVI u32x2 lds4(unsigned a) { return *(const LAS u32x2*)a; }
typedef __amdgpu_buffer_rsrc_t rsrc_t;
DEVI rsrc_t mk_rsrc(const void* p, int bytes) { return __builtin_amdgcn_make_buffer_rsrc((void*)p, 0, bytes, 0x00020000); }
DEVI frag bld16(rsrc_t r, int voff, int soff) { return __builtin_amdgcn_raw_buffer_load_b128(r, voff, soff, 0); }
DEVI u32x2 bld8(rsrc_t r, int voff, int soff) { return __builtin_amdgcn_raw_buffer_load_b64(r, voff, soff, 0); }
DEVI void bst16(rsrc_t r, int voff, int soff, u32x4 v) { __builtin_amdgcn_raw_buffer_store_b128(v, r, voff + soff, 0, 0); }
DEVI void bst16c(rsrc_t r, int voff, int soff, u32x4 v) { __builtin_amdgcn_raw_buffer_store_b128(v, r, voff + soff, 0, 16); }
DEVI void bst8c(rsrc_t r, int voff, int soff, u32x2 v) { __builtin_amdgcn_raw_buffer_store_b64(v, r, voff, soff, 16); }
DEVI u32x2 bld8c(rsrc_t r, int voff, int soff) { return __builtin_amdgcn_raw_buffer_load_b64(r, voff, soff, 16); }
DEVI frag bld16c(rsrc_t r, int voff, int soff) { return __builtin_amdgcn_raw_buffer_load_b128(r, voff, soff, 16); }
DEVI void glds16(const void* base, unsigned voff, unsigned lds_base) {
  unsigned sv;
  asm volatile("s_mov_b32 %0, m0\n\ts_mov_b32 m0, %3\n\ts_nop 0\n\tglobal_load_lds_dwordx4 %1, %2\n\ts_mov_b32 m0, %0"
               : "=&s"(sv) : "v"(voff), "s"(base), "s"(lds_base) : "memory");
}
DEVI void glds16c(const void* base, unsigned voff, unsigned lds_base) {
  unsigned sv;
  asm volatile("s_mov_b32 %0, m0\n\ts_mov_b32 m0, %3\n\ts_nop 0\n\tglobal_load_lds_dwordx4 %1, %2 sc1\n\ts_mov_b32 m0, %0"
               : "=&s"(sv) : "v"(voff), "s"(base), "s"(lds_base) : "memory");
}
DEVI void warm(const void* p, int n8k, int lane, unsigned scratch) {
  for (int i = 0; i < n8k; ++i) {
    unsigned sv;
    asm volatile("s_mov_b32 %0, m0\n\ts_mov_b32 m0, %3\n\ts_nop 0\n\tglobal_load_lds_dword %1, %2\n\ts_mov_b32 m0, %0"
                 : "=&s"(sv) : "v"(lane * 128 + i * 8192), "s"(p), "s"(scratch) : "memory");
  }
}
#define WAITV(n) asm volatile("s_waitcnt vmcnt(%0)" ::"i"(n) : "memory")
#define BAR() asm volatile("s_waitcnt lgkmcnt(0)\n\ts_barrier" ::: "memory")
#define RAWBAR() asm volatile("s_barrier" ::: "memory")
#define SBAR() __builtin_amdgcn_sched_barrier(0)
#define PIN(x) asm volatile("" : "+v"(x))
DEVI int ufl(int x) { return __builtin_amdgcn_readfirstlane(x); }
DEVI void lds_inc(LAS unsigned* w, int lane) { if (lane == 0) __hip_atomic_fetch_add(w, 1u, __ATOMIC_RELAXED, __HIP_MEMORY_SCOPE_WORKGROUP); }
DEVI void signal_done(LAS unsigned* lcnt, unsigned n, GAS u64* flag, u64 tag, int lane) {
  WAITV(0);
  unsigned k = 0;
  if (lane == 0) k = __hip_atomic_fetch_add(lcnt, 1u, __ATOMIC_RELAXED, __HIP_MEMORY_SCOPE_WORKGROUP);
  if ((unsigned)ufl(k) == n - 1 && lane < 16)
    __hip_atomic_store(flag + lane, tag, __ATOMIC_RELAXED, __HIP_MEMORY_SCOPE_AGENT);
}
template <int NAP> DEVI void wait_lds(LAS unsigned* lcnt, unsigned n) {
  unsigned v, cnt = 1u << 22;
  asm volatile("1:\n\tds_read_b32 %0, %2\n\ts_waitcnt lgkmcnt(0)\n\tv_cmp_gt_u32 vcc, %3, %0\n\ts_cbranch_vccz 2f\n\t"
               "s_sleep %4\n\ts_sub_u32 %1, %1, 1\n\ts_cbranch_scc0 1b\n2:"
               : "=&v"(v), "+s"(cnt) : "v"((unsigned)(uintptr_t)lcnt), "s"(n), "i"(NAP) : "vcc", "scc", "memory");
}
template <int NAP> DEVI void wait_flags(GAS u64* grp, int n, u64 tag, int lane) {
  u64 v = lane < n ? ~tag : tag;
#pragma nounroll
  for (unsigned spins = 0; spins < (1u << 21); ++spins) {
    if (v != tag) v = __hip_atomic_load(grp + lane * FSTR, __ATOMIC_RELAXED, __HIP_MEMORY_SCOPE_AGENT);
    if (__all(v == tag)) break;
    __builtin_amdgcn_s_sleep(NAP);
  }
  asm volatile("" ::: "memory");
}
template <int N, int NAP, class F> DEVI void get_mod(unsigned (&v)[N], GAS u64* mod, u64 tag, F&& idx) {
  for (unsigned spins = 0; spins < (1u << 21); ++spins) {
    bool ok = true;
    u64 q[N][4];
#pragma unroll
    for (int i = 0; i < N; ++i)
#pragma unroll
      for (int h = 0; h < 4; ++h) q[i][h] = __hip_atomic_load(mod + h * (6 * D) + idx(i), __ATOMIC_RELAXED, __HIP_MEMORY_SCOPE_AGENT);
#pragma unroll
    for (int i = 0; i < N; ++i) {
      float t = 0.f;
#pragma unroll
      for (int h = 0; h < 4; ++h) { ok &= (q[i][h] >> 24) == (tag >> 24); t += __uint_as_float((unsigned)q[i][h] << 8); }
      t = rbf(t);
      const int sg = idx(i) / D;
      if (sg == 1 || sg == 4) t += 1.0f;
      v[i] = pk2(t, t) & 0xffffu;
    }
    if (__all(ok)) break;
    __builtin_amdgcn_s_sleep(NAP);
  }
}
DEVI u32x4 modulate8(u32x4 w, float rs, float nb, const f32x4& g0, const f32x4& g1, const f32x4& s0, const f32x4& s1) {
  unsigned a = w[0], b = w[1], c = w[2], d = w[3], t0, t1, t2, t3;
  asm("v_lshlrev_b32 %4, 16, %0\n\tv_lshlrev_b32 %5, 16, %1\n\tv_lshlrev_b32 %6, 16, %2\n\tv_lshlrev_b32 %7, 16, %3\n\t"
      "v_and_b32 %0, 0xffff0000, %0\n\tv_and_b32 %1, 0xffff0000, %1\n\tv_and_b32 %2, 0xffff0000, %2\n\tv_and_b32 %3, 0xffff0000, %3\n\t"
      "v_fma_f32 %4, %4, %8, %9\n\tv_fma_f32 %5, %5, %8, %9\n\tv_fma_f32 %6, %6, %8, %9\n\tv_fma_f32 %7, %7, %8, %9\n\t"
      "v_fma_f32 %0, %0, %8, %9\n\tv_fma_f32 %1, %1, %8, %9\n\tv_fma_f32 %2, %2, %8, %9\n\tv_fma_f32 %3, %3, %8, %9\n\t"
      "v_fma_f32 %4, %4, %10, %18\n\tv_fma_f32 %5, %5, %12, %20\n\tv_fma_f32 %6, %6, %14, %22\n\tv_fma_f32 %7, %7, %16, %24\n\t"
      "v_fma_f32 %0, %0, %11, %19\n\tv_fma_f32 %1, %1, %13, %21\n\tv_fma_f32 %2, %2, %15, %23\n\tv_fma_f32 %3, %3, %17, %25\n\t"
      "v_cvt_pk_bf16_f32 %0, %4, %0\n\tv_cvt_pk_bf16_f32 %1, %5, %1\n\tv_cvt_pk_bf16_f32 %2, %6, %2\n\tv_cvt_pk_bf16_f32 %3, %7, %3"
      : "+v"(a), "+v"(b), "+v"(c), "+v"(d), "=&v"(t0), "=&v"(t1), "=&v"(t2), "=&v"(t3)
      : "v"(rs), "v"(nb), "v"(g0[0]), "v"(g0[1]), "v"(g0[2]), "v"(g0[3]), "v"(g1[0]), "v"(g1[1]), "v"(g1[2]), "v"(g1[3]),
        "v"(s0[0]), "v"(s0[1]), "v"(s0[2]), "v"(s0[3]), "v"(s1[0]), "v"(s1[1]), "v"(s1[2]), "v"(s1[3]));
  return u32x4{a, b, c, d};
}
template <int CTRL> DEVI float dpp_add(float v) {
  return v + __builtin_bit_cast(float, __builtin_amdgcn_update_dpp(0, __builtin_bit_cast(int, v), CTRL, 0xf, 0xf, true));
}
DEVI float col4_sum(float v) {
  auto a = __builtin_amdgcn_permlane16_swap(__float_as_uint(v), __float_as_uint(v), false, false);
  v = __uint_as_float(a[0]) + __uint_as_float(a[1]);
  auto b = __builtin_amdgcn_permlane32_swap(__float_as_uint(v), __float_as_uint(v), false, false);
  return __uint_as_float(b[0]) + __uint_as_float(b[1]);
}
DEVI float row16_sum(float v) {
  v = dpp_add<0x128>(v); v = dpp_add<0x124>(v); v = dpp_add<0x122>(v); v = dpp_add<0x121>(v);
  return v;
}
DEVI void quad_part(float& oa, float& ob, const f32x4& a, const f32x4& b) {
  float x, y, z, w;
  asm("s_nop 1\n\t"
      "v_add_f32_dpp %2, %6, %6 row_ror:8 row_mask:0xf bank_mask:0x3\n\t"
      "v_add_f32_dpp %3, %7, %7 row_ror:8 row_mask:0xf bank_mask:0x3\n\t"
      "v_add_f32_dpp %2, %8, %8 row_ror:8 row_mask:0xf bank_mask:0xc\n\t"
      "v_add_f32_dpp %3, %9, %9 row_ror:8 row_mask:0xf bank_mask:0xc\n\t"
      "v_add_f32_dpp %4, %10, %10 row_ror:8 row_mask:0xf bank_mask:0x3\n\t"
      "v_add_f32_dpp %5, %11, %11 row_ror:8 row_mask:0xf bank_mask:0x3\n\t"
      "v_add_f32_dpp %4, %12, %12 row_ror:8 row_mask:0xf bank_mask:0xc\n\t"
      "v_add_f32_dpp %5, %13, %13 row_ror:8 row_mask:0xf bank_mask:0xc\n\t"
      "v_add_f32_dpp %0, %2, %2 row_half_mirror row_mask:0xf bank_mask:0x5\n\t"
      "v_add_f32_dpp %0, %3, %3 row_half_mirror row_mask:0xf bank_mask:0xa\n\t"
      "v_add_f32_dpp %1, %4, %4 row_half_mirror row_mask:0xf bank_mask:0x5\n\t"
      "v_add_f32_dpp %1, %5, %5 row_half_mirror row_mask:0xf bank_mask:0xa"
      : "=&v"(oa), "=&v"(ob), "=&v"(x), "=&v"(y), "=&v"(z), "=&v"(w)
      : "v"(a[0]), "v"(a[1]), "v"(a[2]), "v"(a[3]), "v"(b[0]), "v"(b[1]), "v"(b[2]), "v"(b[3]));
}
DEVI f32x4 gelu4(const f32x4& x) {
  const float c1 = -2.0f * 0.7978845608028654f * 1.4426950408889634f, c3 = c1 * 0.044715f;
  const f32x4 t = x * __builtin_elementwise_fma(x * x, f32x4{c3, c3, c3, c3}, f32x4{c1, c1, c1, c1});
  f32x4 e = {__builtin_amdgcn_exp2f(t[0]), __builtin_amdgcn_exp2f(t[1]), __builtin_amdgcn_exp2f(t[2]), __builtin_amdgcn_exp2f(t[3])};
  e += 1.0f;
  return x * f32x4{__builtin_amdgcn_rcpf(e[0]), __builtin_amdgcn_rcpf(e[1]), __builtin_amdgcn_rcpf(e[2]), __builtin_amdgcn_rcpf(e[3])};
}
DEVI float ln_rs(float var) { return __builtin_amdgcn_rcpf(rbf(__builtin_amdgcn_sqrtf(rbf(rbf(__builtin_fmaxf(var, 0.f)) + 1e-5f)))); }
DEVI f32x4 splat(float v) { return f32x4{v, v, v, v}; }
DEVI u32x4 gated(const u32x4& res, const u32x4& gate, const f32x4& ce, const f32x4& co) {
  return pkI(upE(res) + upE(gate) * ce, upO(res) + upO(gate) * co);
}
DEVI u32x2 gated4(const u32x2& res, const u32x2& gate, const f32x4& c) { return pk4(up4(res[0], res[1]) + up4(gate[0], gate[1]) * c); }
DEVI int prow(int mt, int r) { return 32 * (mt >> 1) + 8 * (r >> 2) + 4 * (mt & 1) + (r & 3); }

DEVI void rows8(unsigned* r, rsrc_t w, int voff, int pitch, int k0, int k1) {
#pragma unroll
  for (int i = 0; i < 8; ++i) r[i] = __builtin_amdgcn_raw_buffer_load_b32(w, voff, ((i < 4 ? k0 : k1 - 4) + i) * pitch, 2);
}
DEVI void arrange(int& d0, int& d1, bool nat, int lane, u32x4& c0, u32x4& c1) {
  const int dm = __builtin_amdgcn_update_dpp(0, d1, 0x141, 0xf, 0xf, true);
  const bool up = nat && (lane & 4);
#pragma unroll
  for (int i = 0; i < 4; ++i) {
    const unsigned m = __builtin_amdgcn_update_dpp(0, (int)c1[i], 0x141, 0xf, 0xf, true), a = c0[i], b = nat ? m : c1[i];
    c0[i] = up ? b : a; c1[i] = up ? a : b;
  }
  if (nat) d1 = dm;
  const int t = d0; d0 = up ? d1 : t; d1 = up ? t : d1;
}
DEVI void transp(u32x4& c0, u32x4& c1, const unsigned* r) {
#pragma unroll
  for (int i = 0; i < 4; ++i) {
    c0[i] = __builtin_amdgcn_perm(r[2 * i + 1], r[2 * i], 0x05040100u);
    c1[i] = __builtin_amdgcn_perm(r[2 * i + 1], r[2 * i], 0x07060302u);
  }
}
DEVI int ncol(int o, int npair) { return o < 32 * npair ? (o & ~31) | ((o & 1) << 4) | ((o >> 1) & 15) : o; }
template <int W, int NP> DEVI void krows(int ks, int g, int& k0, int& k1, bool& ok) {
  ok = true;
  if (ks < 8 * NP) { const int j = ks / NP; k0 = W * j + 32 * (ks - j * NP) + 8 * g; k1 = k0 + 4; }
  else { k0 = W * 2 * (ks - 8 * NP) + 32 * NP + 4 * g; k1 = k0 + W; ok = 32 * NP + 4 * g < W; }
}
DEVI void tile_proj(unsigned* r, int& d0, int& d1, const u16* __restrict__ wo, const u16* __restrict__ w_out, int f, int j, int lane) {
  const bool o = f < 80;
  const int k8 = o ? f : f - 80, oc = 2 * lane;
  int k0, k1; bool ok = true;
  if (!o) krows<108, 3>(k8 >> 2, k8 & 3, k0, k1, ok);
  else if (k8 < 64) { k0 = DH * (k8 >> 1) + 4 * (k8 & 1); k1 = k0 + 8; }
  else { k0 = 2 * DH * (k8 - 64) + 16; k1 = k0 + DH; }
  rows8(r, mk_rsrc(o ? wo : w_out, ok ? (o ? D : FH) * D * 2 : 0), lane < 40 ? (80 * j + oc) * 2 : (1 << 28), D * 2, k0, k1);
  const int c = ncol(oc, 2);
  d0 = lane < 40 ? (o ? WS_WO : WS_WOUT) + ((j * (o ? 20 : 28) + (k8 >> 2)) * 5 + (c >> 4)) * 1024 + ((k8 & 3) * 16 + (c & 15)) * 16 : WS_END;
  d1 = d0 + (oc < 64 ? 1024 : 16);
}

struct Gemv {
  u32x4 mw[5]; unsigned cw[5];
  DEVI void load(const u16* w_mod, const u16* cvec, int u, int w, int lane) {
    const rsrc_t mr = mk_rsrc(w_mod + (u & 3) * 160 * (6 * D), 160 * 6 * D * 2), cr = mk_rsrc(cvec + (u & 3) * 160, 320);
#pragma unroll
    for (int i = 0; i < 5; ++i) {
      const int k = w * 8 + (lane >> 3) + 32 * i;
      mw[i] = __builtin_amdgcn_raw_buffer_load_b128(mr, k * (6 * D * 2) + (lane & 7) * 16, (u >> 2) * 128, 2);
      cw[i] = __builtin_amdgcn_raw_buffer_load_b16(cr, k * 2, 0, 0);
    }
  }
  DEVI void finish(GAS u64* mod, u64 tag, int u, unsigned red, LAS unsigned* cnt, int w, int lane) {
    float a[8] = {};
#pragma unroll
    for (int i = 0; i < 5; ++i) {
      const float cx = bf_lo(cw[i]);
      const float sc = rbf(cx * __builtin_amdgcn_rcpf(1.0f + __builtin_amdgcn_exp2f(-1.4426950408889634f * cx)));
#pragma unroll
      for (int e = 0; e < 4; ++e) { a[2 * e] += sc * bf_lo(mw[i][e]); a[2 * e + 1] += sc * bf_hi(mw[i][e]); }
    }
#pragma unroll
    for (int e = 0; e < 8; ++e) a[e] = col4_sum(dpp_add<0x128>(a[e]));
    if (lane < 8) {
      *(LAS f32x4*)(red + w * 256 + lane * 32) = f32x4{a[0], a[1], a[2], a[3]};
      *(LAS f32x4*)(red + w * 256 + lane * 32 + 16) = f32x4{a[4], a[5], a[6], a[7]};
    }
    asm volatile("s_waitcnt lgkmcnt(0)" ::: "memory");
    unsigned last = 0;
    if (lane == 0) last = __hip_atomic_fetch_add(cnt, 1u, __ATOMIC_RELAXED, __HIP_MEMORY_SCOPE_WORKGROUP);
    if (ufl(last) == 3) {
      asm volatile("" ::: "memory");
      float sm = 0.f;
#pragma unroll
      for (int i = 0; i < 4; ++i) sm += *(const LAS float*)(red + i * 256 + lane * 4);
      __hip_atomic_store(mod + (u & 3) * (6 * D) + (u >> 2) * 64 + lane, (tag & ~0xffffffull) | ((__float_as_uint(sm) + 0x80u) >> 8),
                         __ATOMIC_RELAXED, __HIP_MEMORY_SCOPE_AGENT);
    }
  }
};

constexpr int Q_GS = 129024, Q_RED = Q_GS + 5120, Q_PM = Q_RED + 1024, Q_V = Q_PM + 256;
DEVI unsigned afrag_base(unsigned lds0, int lane, int odd) {
  const int r = lane & 15, g = lane >> 4;
  return lds0 + r * 128 + (((4 * odd + g) ^ (r & 7)) << 4);
}
constexpr int Q_AHEAD = 6;
template <bool VW, int NT>
DEVI void qkv_mma(char* __restrict__ ws, unsigned lds0, LAS unsigned* rdy, int wv, int pi, int j) {
  const int lane = TID & 63, r = lane & 15, g = lane >> 4;
  const rsrc_t wr = mk_rsrc(ws + WS_WQKV + (j * 15 + 2 * wv) * 20 * 1024, wv < 7 ? 40 * 1024 : 20 * 1024);
  frag bw[20][2], a[2][4];
  auto ldb = [&](int ks) { if (ks < 20) { bw[ks][0] = bld16c(wr, lane * 16, ks * 1024); if (NT > 1) bw[ks][1] = bld16c(wr, lane * 16, (20 + ks) * 1024); } };
#pragma unroll
  for (int ks = 0; ks < Q_AHEAD; ++ks) ldb(ks);
  SBAR();
  f32x4 acc[2][4] = {};
  unsigned a0 = afrag_base(lds0, lane, 0), a1 = afrag_base(lds0, lane, 1), a2 = a0 + 40960, a3 = a1 + 40960;
  PIN(a0); PIN(a1); PIN(a2); PIN(a3);
  wait_lds<1>(rdy, 4u);
#pragma unroll
  for (int mt = 0; mt < 4; ++mt) a[0][mt] = lds8(a0 + mt * 2048);
#pragma unroll
  for (int ks = 0; ks < 20; ++ks) {
    const int kl = ks + 1;
    if (kl < 20 && (kl & 3) == 0) wait_lds<1>(rdy + (kl >> 2), 4u);
#pragma unroll
    for (int mt = 0; mt < 4; ++mt) {
      if (kl < 20) a[kl & 1][mt] = lds8((kl < 10 ? ((kl & 1) ? a1 : a0) : ((kl & 1) ? a3 : a2)) + ((kl % 10) >> 1) * 8192 + mt * 2048);
      SBAR();
#pragma unroll
      for (int t = 0; t < NT; ++t)
        acc[t][mt] = VW ? mfma16(a[ks & 1][mt], bw[ks][t], acc[t][mt]) : mfma16(bw[ks][t], a[ks & 1][mt], acc[t][mt]);
      SBAR();
    }
    ldb(ks + Q_AHEAD);
    SBAR();
  }
  const rsrc_t wsr = mk_rsrc(ws, WS_END);
  if (!VW) {
    const float qs = 0.22360679774997896f * 1.4426950408889634f;
    if (wv < 4) {
      const float sc = wv < 2 ? qs : 1.0f;
      const int voff = (8 * j + 4 * (wv & 1) + g) * (S * 16) + (pi * 64 + r) * 16;
#pragma unroll
      for (int mt = 0; mt < 4; ++mt) bst16c(wsr, voff, (wv < 2 ? WS_QA : WS_KA) + mt * 256, pkI(acc[0][mt] * sc, acc[1][mt] * sc));
    } else {
      const int voff = (4 * j + g) * (S * 8) + pi * 512 + r * 16;
#pragma unroll
      for (int h = 0; h < 2; ++h) {
        bst16c(wsr, voff, WS_QB + h * 256, pk8(acc[0][2 * h] * qs, acc[0][2 * h + 1] * qs));
        bst16c(wsr, voff, WS_KB + h * 256, pk8(acc[1][2 * h], acc[1][2 * h + 1]));
      }
    }
  } else {
#pragma unroll
    for (int t = 0; t < NT; ++t) {
      const int o = 16 * (2 * wv + t - 10) + r, hh = o / DH, d = o - hh * DH;
      const unsigned va = lds0 + Q_V + ((hh * 8 + g) * NV + d) * 16;
      *(LAS u32x4*)va = pk8(acc[t][0], acc[t][1]);
      *(LAS u32x4*)(va + 4 * NV * 16) = pk8(acc[t][2], acc[t][3]);
    }
  }
}

template <int NS, int NF>
DEVI void proj_mma(f32x4 (&acc)[5], rsrc_t ar, int vt, int hioff, unsigned lds0, int hw, int lane) {
  const int vf = lane * 16 + hw * 1024;
  frag a[NF], b[3][5]; u32x2 at[NS - NF][2];
  auto lda = [&](int i) {
    if (i < NF) a[i] = bld16c(ar, vf, i * 4096);
    else if (i < NS) { at[i - NF][0] = bld8c(ar, vt, i * 4096); at[i - NF][1] = bld8c(ar, vt, i * 4096 + hioff); }
  };
  auto upto = [](int ks) { return ks < 2 ? 12 + 3 * ks : ks + 16; };
#pragma unroll
  for (int i = 0; i < upto(0); ++i) { lda(i); SBAR(); }
  unsigned bb = lds0 + lane * 16, bb1 = bb + 61440, bb2 = bb + 122880;
  PIN(bb); PIN(bb1); PIN(bb2);
#pragma unroll
  for (int t = 0; t < 5; ++t) acc[t] = f32x4{};
#pragma unroll
  for (int t = 0; t < 10; ++t) b[t / 5][t % 5] = lds8(bb + t * 1024);
#pragma unroll
  for (int ks = 0; ks < NS; ++ks) {
    const int kl = ks + 2;
    const frag av = ks < NF ? a[ks < NF ? ks : 0] : frag{at[ks < NF ? 0 : ks - NF][0][0], at[ks < NF ? 0 : ks - NF][0][1], at[ks < NF ? 0 : ks - NF][1][0], at[ks < NF ? 0 : ks - NF][1][1]};
#pragma unroll
    for (int t = 0; t < 5; ++t) {
      if (kl < NS) b[kl % 3][t] = lds8((kl < 12 ? bb : (kl < 24 ? bb1 : bb2)) + (kl % 12) * 5120 + t * 1024);
      SBAR();
      acc[t] = mfma16(b[ks % 3][t], av, acc[t]);
      SBAR();
    }
#pragma unroll
    for (int i = upto(ks); i < upto(ks + 1); ++i) lda(i);
    SBAR();
  }
}

template <int NT, bool FIRST>
DEVI void sp_loop(f32x4 (&acc)[2][4], rsrc_t br, int voff, unsigned ab, int rot) {
  constexpr int W = 4;
  frag bw[W][2], a[2][4];
  auto issue = [&](int kb, int i) {
    const int o = ((kb + rot) & 31) * 2048;
    bw[i][0] = bld16c(br, voff, o);
    if (NT > 1) bw[i][1] = bld16c(br, voff + 65536, o);
  };
#pragma unroll
  for (int i = 0; i < W - (FIRST ? 1 : 0); ++i) issue(i, i);
#pragma unroll
  for (int mt = 0; mt < 4; ++mt) a[0][mt] = lds8(ab + mt * 2048);
  auto window = [&](int k0, bool last) {
    const unsigned ac = ab + (k0 & 15) * 8192, an = ab + ((k0 + W) & 15) * 8192;
#pragma unroll
    for (int sl = 0; sl < W; ++sl) {
      if ((sl & 3) == 0) RAWBAR();
      if (FIRST && (!last || sl == 0)) { issue(k0 + sl + W - 1, (sl + W - 1) % W); SBAR(); }
#pragma unroll
      for (int mt = 0; mt < 4; ++mt) {
        if (!last || sl + 1 < W) a[(sl + 1) & 1][mt] = lds8((sl + 1 < W ? ac + (sl + 1) * 8192 : an) + mt * 2048);
        SBAR();
#pragma unroll
        for (int t = 0; t < NT; ++t) acc[t][mt] = mfma16(bw[sl][t], a[sl & 1][mt], acc[t][mt]);
        SBAR();
      }
      if (!FIRST && !last) { issue(k0 + sl + W, sl); SBAR(); }
    }
  };
#pragma nounroll
  for (int k0 = 0; k0 < 32 - W; k0 += W) window(k0, false);
  window(32 - W, true);
}

constexpr int AT_C = 512;
constexpr int AT_KB = 2 * AT_C * 16, AT_V = AT_KB + AT_C * 8, AT_SLOT = AT_V + (AT_C / 8) * NV * 16;
constexpr int AT_RED = 3 * AT_SLOT, M_ZN = AT_RED + 8 * 3072, M_LN = M_ZN + 512, M_UV = M_LN + 512;
constexpr int M_GATE = 156 * 1024, AT_SCR = M_GATE + 320, AT_SYNC = AT_SCR + 256, AT_LDS = 160 * 1024;
constexpr int M_IMG = 48 * 1024, G_RED = 128 * 1024, G_OUT = 140 * 1024;
constexpr int FL_WQ = 0, FL_QKV = 8, FL_PREP = 16, FL_O = 24, FL_Z = 32, FL_X1 = 40, FL_U = 72;
DEVI float max16(const f32x16& p) {
  float a = __builtin_fmaxf(__builtin_fmaxf(p[0], p[1]), p[2]), b = __builtin_fmaxf(__builtin_fmaxf(p[3], p[4]), p[5]);
  a = __builtin_fmaxf(__builtin_fmaxf(a, p[6]), p[7]); b = __builtin_fmaxf(__builtin_fmaxf(b, p[8]), p[9]);
  a = __builtin_fmaxf(__builtin_fmaxf(a, p[10]), p[11]); b = __builtin_fmaxf(__builtin_fmaxf(b, p[12]), p[13]);
  a = __builtin_fmaxf(__builtin_fmaxf(a, p[14]), p[15]);
  float m = __builtin_fmaxf(a, b);
  auto rr = __builtin_amdgcn_permlane32_swap(__float_as_uint(m), __float_as_uint(m), false, false);
  return __builtin_fmaxf(__uint_as_float(rr[0]), __uint_as_float(rr[1]));
}
DEVI frag pack8(const f32x16& p, int o) {
  return frag{pk2(p[o], p[o + 1]), pk2(p[o + 2], p[o + 3]), pk2(p[o + 4], p[o + 5]), pk2(p[o + 6], p[o + 7])};
}
struct Prep {
  unsigned r[32]; u32x4 c[8]; int d[8];
  DEVI void load(const u16* w_in, const u16* wo, const u16* w_out, int f, int j, int lane) {
    if (f < 80) {
      const int k16 = f >> 1, o = 2 * lane;
      int k0[2], k1[2];
#pragma unroll
      for (int h = 0; h < 2; ++h) {
        bool ok; krows<80, 2>(k16 >> 1, 2 * (k16 & 1) + h, k0[h], k1[h], ok);
        rows8(r + 8 * h, mk_rsrc(w_in, D * FF * 2), o < 108 ? (((f & 1) ? 0 : FH) + 108 * j + o) * 2 : (1 << 28), FF * 2, k0[h], k1[h]);
      }
    } else {
#pragma unroll
      for (int i = 0; i < 4; ++i) tile_proj(r + 8 * i, d[2 * i], d[2 * i + 1], wo, w_out, (f - 80) * 4 + i, j, lane);
    }
  }
  DEVI void compute(unsigned mv, int f, int j, int lane) {
    if (f < 80) {
      const int k16 = f >> 1, seg = f & 1, o = 2 * lane;
      const int mf = mv << 16;
      const int n0 = ncol(o, 3), db = WS_WIN + ((j * 14 + seg * 7) * 20 + (k16 >> 1)) * 1024 + (k16 & 1) * 512;
      float u0 = 0.f, u1 = 0.f, v0 = 0.f, v1 = 0.f;
#pragma unroll
      for (int h = 0; h < 2; ++h) {
#pragma unroll
        for (int i = 0; i < 8; i += 2) {
          const float ga = __int_as_float(__builtin_amdgcn_readlane(mf, 8 * h + i)), gb = __int_as_float(__builtin_amdgcn_readlane(mf, 8 * h + i + 1));
          const float sa = __int_as_float(__builtin_amdgcn_readlane(mf, 16 + 8 * h + i)), sb = __int_as_float(__builtin_amdgcn_readlane(mf, 17 + 8 * h + i));
          const float a0 = bf_lo(r[8 * h + i]), a1 = bf_hi(r[8 * h + i]), b0 = bf_lo(r[8 * h + i + 1]), b1 = bf_hi(r[8 * h + i + 1]);
          const unsigned w0 = pk2(a0 * ga, b0 * gb), w1 = pk2(a1 * ga, b1 * gb);
          c[2 * h][i >> 1] = w0; c[2 * h + 1][i >> 1] = w1;
          u0 = dot2(w0, 0x3f803f80u, u0); u1 = dot2(w1, 0x3f803f80u, u1);
          v0 = __builtin_fmaf(a0, sa, __builtin_fmaf(b0, sb, v0)); v1 = __builtin_fmaf(a1, sa, __builtin_fmaf(b1, sb, v1));
        }
        d[2 * h] = lane < 56 ? db + (n0 >> 4) * 20480 + (n0 & 15) * 16 + h * 256 : WS_END;
        d[2 * h + 1] = d[2 * h] + (o < 96 ? 20480 : 16);
        arrange(d[2 * h], d[2 * h + 1], o >= 96, lane, c[2 * h], c[2 * h + 1]);
      }
      c[4] = __builtin_bit_cast(u32x4, f32x4{u0, v0, u1, v1});
      d[4] = lane < 56 ? WS_UVP + ((j * 40 + k16) * 224 + seg * 112 + o) * 8 : WS_END;
#pragma unroll
      for (int i = 5; i < 8; ++i) { c[i] = c[4]; d[i] = WS_END; }
    } else {
#pragma unroll
      for (int i = 0; i < 4; ++i) { transp(c[2 * i], c[2 * i + 1], r + 8 * i); arrange(d[2 * i], d[2 * i + 1], lane >= 32, lane, c[2 * i], c[2 * i + 1]); }
    }
#pragma unroll
    for (int i = 0; i < 8; ++i) { PIN(c[i]); PIN(d[i]); }
  }
  DEVI void store(rsrc_t wsr, int f) {
#pragma unroll
    for (int i = 0; i < 8; ++i) if (i < 5 || f >= 80) bst16c(wsr, d[i], 0, c[i]);
  }
};
__global__ __launch_bounds__(768, 3) void k_all(const u16* __restrict__ w_mod, const u16* __restrict__ cvec, const u16* __restrict__ wqkv,
                                                const u16* __restrict__ x, char* __restrict__ ws, const u16* __restrict__ w_in,
                                                const u16* __restrict__ wo, const u16* __restrict__ w_out, const u16* __restrict__ w_sp,
                                                const u16* __restrict__ b_sp, u16* __restrict__ out) {
  asm volatile("" : "+s"(w_mod), "+s"(cvec), "+s"(wqkv), "+s"(x), "+s"(ws), "+s"(w_sp));
  extern __shared__ __attribute__((aligned(16))) char smem[];
  const int tid = TID, lane = tid & 63, hw = ufl(tid >> 6), wv = hw - 4, r32 = lane & 31, hi = lane >> 5, r = lane & 15, g = lane >> 4;
  const int p2 = ((BID & 7) >> 1) * 8 + ((BID >> 3) & 7), j2 = (BID & 1) * 4 + (BID >> 6);
  const int head = (BID & 7) * 4 + ((BID >> 3) & 3), qb = BID >> 5;
  const int pi = (BID & 7) * 4 + ((BID >> 3) & 3), j = BID >> 5;
  const int rot = 4 * j;
  const unsigned lds0 = (unsigned)(uintptr_t)smem;
  LAS unsigned* sync = (LAS unsigned*)(lds0 + AT_SYNC);
  if (tid < 32) sync[tid] = 0u;
  BAR();
  {
    const char* pc0;
    asm volatile("s_getpc_b64 %0" : "=s"(pc0));
    const int ci = ((BID >> 3) & 1) * 4 + hw;
    if (hw < 4 && BID >= 240) warm(pc0 + ci * 8192, 1, lane, lds0 + AT_SCR);
  }
  Gemv gv0;
  if (hw < 4 && BID < 240) gv0.load(w_mod, cvec, BID, hw, lane);
  u64 tag = (dispatch_id_() + 0x632BE59BD9B4E019ull) * 0x9E3779B97F4A7C15ull ^ (u64)__builtin_amdgcn_dispatch_ptr() * 0xC2B2AE3D27D4EB4Full;
  tag ^= tag >> 29; tag *= 0xBF58476D1CE4E5B9ull; tag ^= tag >> 32;
  GAS u64* flags = (GAS u64*)(ws + WS_FLAG);
#define FP(grp, m) (flags + ((grp) * 32 + (m)) * FSTR)
  GAS u64* mod = (GAS u64*)(ws + WS_MOD);
  if (hw < 4 && BID < 240) gv0.finish(mod, tag, BID, lds0 + Q_RED, sync + 17, hw, lane);
  LAS float* znp = (LAS float*)(lds0 + M_ZN);
  const unsigned partb = lds0 + G_RED;
  LAS float* lnp = (LAS float*)(lds0 + M_LN);
  LAS float* uvp = (LAS float*)(lds0 + M_UV);
  const rsrc_t wsr = mk_rsrc(ws, WS_END);
  const char* KA = ws + WS_KA + head * (S * 32);
  const char* KB = ws + WS_KB + head * (S * 8);
  const char* VT = ws + WS_VT + head * (256 * NV * 16);
  if (hw < 4) {
    const int pf = (BID & 31) * 4 + hw;
    Prep pp;
    {
      u32x4 xv[4][5]; float rs[4], nb[4];
      const rsrc_t xr = mk_rsrc(x + (size_t)(p2 * 64 + hw * 16) * D, 16 * D * 2);
#pragma unroll
      for (int p = 0; p < 4; ++p)
#pragma unroll
        for (int i = 0; i < 5; ++i) xv[p][i] = __builtin_amdgcn_raw_buffer_load_b128(xr, g * (D * 2) + r * 16, p * 4 * D * 2 + i * 256, 2);
      if (hw == 1) {
        const unsigned cv = (lane & 1) ? 0u : 0x3f803f80u;
        *(LAS u32x4*)(lds0 + Q_V + ((lane >> 1) * NV + DH + (lane & 1)) * 16) = u32x4{cv, cv, cv, cv};
      }
      if (hw == 2) bst8c(wsr, lane * 8, WS_STAT + (pi * 8 + j) * 512, u32x2{~0u, ~0u});
#pragma unroll
      for (int p = 0; p < 4; ++p) {
        float s1 = 0.f, s2 = 0.f;
#pragma unroll
        for (int i = 0; i < 5; ++i)
#pragma unroll
          for (int e = 0; e < 4; ++e) { const unsigned w = xv[p][i][e]; s1 = dot2(w, 0x3f803f80u, s1); s2 = dot2(w, w, s2); }
        const float mu = row16_sum(s1) * (1.0f / D);
        rs[p] = ln_rs(row16_sum(s2) * (1.0f / D) - mu * mu);
        nb[p] = -rbf(mu) * rs[p];
        PIN(rs[p]); PIN(nb[p]);
      }
      WAITV(0);
      wait_lds<1>(sync + 13, 5u);
      __builtin_amdgcn_s_setprio(LNP);
      unsigned gsa = lds0 + Q_GS + r * 32;
      PIN(gsa);
      f32x4 gs[2][4];
      auto ldgs = [&](int i) {
        gs[i & 1][0] = *(const LAS f32x4*)(gsa + i * 512); gs[i & 1][1] = *(const LAS f32x4*)(gsa + i * 512 + 16);
        gs[i & 1][2] = *(const LAS f32x4*)(gsa + 2560 + i * 512); gs[i & 1][3] = *(const LAS f32x4*)(gsa + 2560 + i * 512 + 16);
      };
      ldgs(0);
      unsigned wa[2];
#pragma unroll
      for (int h = 0; h < 2; ++h) {
        const int ri = 8 * (hw & 1) + 4 * h + g;
        wa[h] = lds0 + (r >> 3) * 8192 + ((hw >> 1) * 32 + ri) * 128 + (((r & 7) ^ (ri & 7)) << 4);
      }
#pragma unroll
      for (int i = 0; i < 5; ++i) {
        if (i < 4) ldgs(i + 1);
#pragma unroll
        for (int p = 0; p < 4; ++p) {
          *(LAS u32x4*)(wa[p >> 1] + i * 16384 + (p & 1) * 2048) = modulate8(xv[p][i], rs[p], nb[p], gs[i & 1][2], gs[i & 1][3], gs[i & 1][0], gs[i & 1][1]);
        }
        asm volatile("s_waitcnt lgkmcnt(0)" ::: "memory");
        lds_inc(sync + 24 + i, lane);
      }
      __builtin_amdgcn_s_setprio(2);
      wait_lds<1>(sync + 21, 3u);
#pragma unroll
      for (int i = 0; i < 3; ++i)
        if (i * 64 + lane < 8 * NV) bst16c(wsr, ((4 * j2 + hw) * 256 + p2 * 8) * (NV * 16) + (i * 64 + lane) * 16, WS_VT,
                                           lds8(lds0 + Q_V + (hw * 8 * NV + i * 64 + lane) * 16));
      signal_done(sync + 16, 12u, FP(FL_QKV + j2, p2), tag, lane);
    }
    __builtin_amdgcn_s_setprio(2);
    auto stage = [&](int ch) {
      const unsigned sl = lds0 + ((ch + 2) % 3) * AT_SLOT;
#pragma unroll
      for (int i = 0; i < 2; ++i) {
        glds16c(KA + ch * (AT_C * 16) + (hw + 4 * i) * 1024, lane * 16, sl + (hw + 4 * i) * 1024);
        glds16c(KA + (S + ch * AT_C) * 16 + (hw + 4 * i) * 1024, lane * 16, sl + AT_C * 16 + (hw + 4 * i) * 1024);
      }
      glds16c(KB + ch * (AT_C * 8) + hw * 1024, lane * 16, sl + AT_KB + hw * 1024);
#pragma unroll
      for (int i = 0; i < 6; ++i) {
        const int pc = min(hw + 4 * i, NV - 1);
        glds16c(VT + ch * (NV * 1024) + pc * 1024, lane * 16, sl + AT_V + pc * 1024);
      }
    };
    if (hw == 0) { wait_lds<2>(sync + 16, 12u); __builtin_amdgcn_s_sleep(DQ); wait_flags<4>(FP(FL_QKV + (BID & 7), 0), 32, tag, lane); lds_inc(sync + 14, lane); }
    else wait_lds<1>(sync + 14, 1u);
    stage(0);
    WAITV(6); lds_inc(sync + 20, lane);
    WAITV(0); lds_inc(sync, lane);
    wait_lds<1>(sync + 16, 12u);
    stage(1); stage(2);
    WAITV(11); lds_inc(sync + 1, lane);
    WAITV(0); lds_inc(sync + 2, lane);
    const unsigned WSCR = lds0 + AT_SCR;
    pp.load(w_in, wo, w_out, pf, j, lane);
    wait_lds<2>(sync + 19, 1u);
    pp.compute(*(const LAS u16*)(lds0 + Q_PM + (hw * 32 + r32) * 2), pf, j, lane);
    pp.store(wsr, pf);
    signal_done(sync + 7, 4u, FP(FL_PREP + j, (BID & 31)), tag, lane);
    warm((const char*)w_sp + ((BID & 7) * 128 + (BID >> 3) * 4 + hw) * 8192, 1, lane, WSCR);
    wait_lds<8>(sync + 4, 8u);
    stage(3);
    WAITV(0); lds_inc(sync + 3, lane);
    wait_flags<8>(FP(FL_PREP + j, 0), 32, tag, lane);
    {
      const int o = 56 * (hw & 1) + lane, seg = hw >> 1;
      const int vo = lane < 56 ? (j * 40 * 224 + seg * 112 + o) * 8 : WS_END;
      float u = 0.f, v = 0.f;
#pragma unroll
      for (int h = 0; h < 2; ++h) {
        u32x2 t[20];
#pragma unroll
        for (int i = 0; i < 20; ++i) t[i] = bld8c(wsr, vo, WS_UVP + (h * 20 + i) * 224 * 8);
#pragma unroll
        for (int i = 0; i < 20; ++i) { u += __uint_as_float(t[i][0]); v += __uint_as_float(t[i][1]); }
      }
      if (lane < 56) { uvp[seg * 112 + ncol(o, 3)] = u; uvp[224 + seg * 112 + ncol(o, 3)] = v; }
    }
    const char* wsrc = ws + WS_WO + (j * 100 + hw) * 1024;
    auto issue_w = [&](int n0, int n1) {
#pragma unroll
      for (int n = n0; n < n1; ++n) glds16c(wsrc + n * 4096, lane * 16, lds0 + hw * 1024 + n * 4096);
    };
    frag wl[4]; u32x4 r0, r1; u32x2 rt;
#pragma unroll
    for (int n = 0; n < 4; ++n) wl[n] = bld16c(wsr, lane * 16, WS_WO + (j * 100 + 84 + 4 * n + hw) * 1024);
    {
      const rsrc_t xr = mk_rsrc(x + (size_t)pi * 64 * D + 80 * j, 64 * D * 2 - 160 * j);
      const int xoff = prow(hw, r) * (D * 2);
      r0 = bld16(xr, xoff + g * 16, 0); r1 = bld16(xr, xoff + g * 16, 64); rt = bld8(xr, xoff + g * 8, 128);
    }
    wait_lds<8>(sync + 5, 8u); issue_w(0, 10);
    wait_lds<8>(sync + 6, 8u); issue_w(10, 21);
    warm((const char*)w_sp + ((BID & 7) * 128 + (BID >> 3) * 4 + hw) * 8192, 1, lane, WSCR);
    WAITV(0);
    BAR();
#pragma unroll
    for (int n = 0; n < 4; ++n) *(LAS frag*)(lds0 + (84 + 4 * n + hw) * 1024 + lane * 16) = wl[n];
    asm volatile("s_waitcnt lgkmcnt(0)" ::: "memory");
    RAWBAR();
    const int xo = WS_X1 + pi * 81920;
    u32x4 y0, y1; u32x2 yt;
    {
      f32x4 c[5];
      proj_mma<20, 16>(c, mk_rsrc(ws + WS_O + pi * 81920, 81920), g * 1024 + (hw >> 1) * 256 + r * 16 + (hw & 1) * 8, 512, lds0, hw, lane);
      y0 = gated(r0, lds8(lds0 + M_GATE + g * 16), c[0], c[1]); y1 = gated(r1, lds8(lds0 + M_GATE + 64 + g * 16), c[2], c[3]);
      yt = gated4(rt, lds4(lds0 + M_GATE + 128 + g * 8), c[4]);
      bst16c(wsr, lane * 16, xo + 2 * j * 4096 + hw * 1024, y0);
      bst16c(wsr, lane * 16, xo + (2 * j + 1) * 4096 + hw * 1024, y1);
      bst8c(wsr, lane * 8, xo + 65536 + j * 2048 + hw * 512, yt);
      float s1 = 0.f, s2 = 0.f, s3 = 0.f, s4 = 0.f;
#pragma unroll
      for (int e = 0; e < 5; ++e) {
        const unsigned v0 = e < 4 ? y0[e & 3] : yt[0], v1 = e < 4 ? y1[e & 3] : yt[1];
        s1 = dot2(v0, 0x3f803f80u, s1); s2 = dot2(v0, v0, s2); s3 = dot2(v1, 0x3f803f80u, s3); s4 = dot2(v1, v1, s4);
      }
      s1 = col4_sum(s1 + s3); s2 = col4_sum(s2 + s4);
      if (g == 0) bst8c(wsr, r * 8, WS_XST + ((pi * 8 + j) * 4 + hw) * 128, u32x2{__float_as_uint(s1), __float_as_uint(s2)});
      signal_done(sync + 9, 4u, FP(FL_X1 + pi, j), tag, lane);
    }
    RAWBAR();
    {
      auto ldx = [&](int n0, int n1) {
#pragma unroll
        for (int n = n0; n < n1; ++n) glds16c(ws + xo + n * 4096 + hw * 1024, lane * 16, lds0 + M_IMG + n * 4096 + hw * 1024);
      };
      ldx(0, 12);
      WAITV(8); RAWBAR();
      ldx(12, 20);
      WAITV(8); RAWBAR();
      WAITV(0); RAWBAR();
    }
    {
      const int rr = lane >> 3;
      const char* src = (const char*)w_sp + (size_t)(pi * 64 + prow(hw, 0)) * (S * 2);
      const unsigned sv0 = prow(0, rr) * (S * 2) + (((lane & 7) ^ rr) << 4), dst = lds0 + hw * 2048;
      auto issue = [&](int kb) {
        const char* sp = src + ((kb + rot) & 31) * 128;
        const unsigned d = dst + (kb & 15) * 8192;
        glds16(sp, sv0, d); glds16(sp + 16 * (S * 2), sv0, d + 1024);
      };
      const char* osrc = ws + WS_WOUT + (j * 140 + hw) * 1024;
      auto issue_o = [&](int n0, int n1) {
#pragma unroll
        for (int n = n0; n < n1; ++n) glds16c(osrc + n * 4096, lane * 16, lds0 + hw * 1024 + n * 4096);
      };
      __builtin_amdgcn_s_setprio(0);
#pragma nounroll
      for (int kb = 0; kb < 6; ++kb) issue(kb);
      frag park[32];
      {
        const rsrc_t sr = mk_rsrc(src, 32 * S * 2);
        const int pv = prow(0, rr) * (S * 2) + (lane & 7) * 16;
#pragma unroll
        for (int i = 0; i < 32; ++i) park[i] = bld16(sr, pv + (i & 1) * 16 * (S * 2), ((16 + (i >> 1) + rot) & 31) * 128);
      }
      __builtin_amdgcn_s_setprio(2);
      RAWBAR();
      RAWBAR();
#pragma nounroll
      for (int kb = 6; kb < 16; ++kb) issue(kb);
      WAITV(20);
      RAWBAR();
      RAWBAR();
      unsigned pw = dst + rr * 128 + (((lane & 7) ^ rr) << 4), pw2 = pw + 65536;
      PIN(pw); PIN(pw2);
#pragma unroll
      for (int m = 1; m < 5; ++m) {
        if (m == 1) WAITV(12); else if (m == 2) WAITV(0);
        BAR();
#pragma unroll
        for (int i = 8 * m - 8; i < 8 * m; ++i) *(LAS frag*)((i < 16 ? pw : pw2) + ((i >> 1) & 7) * 8192 + (i & 1) * 1024) = park[i];
      }
      BAR(); RAWBAR();
      RAWBAR(); __builtin_amdgcn_s_sleep(4); issue_o(0, 16);
      BAR();
      wait_lds<1>(sync + 12, 8u);
      issue_o(16, 35);
      WAITV(0);
      RAWBAR();
      f32x4 c[5];
      proj_mma<28, 24>(c, mk_rsrc(ws + WS_U + pi * (28 * 4096), 28 * 4096), lane * 16 + (hw >> 1) * 1024 + (hw & 1) * 8, 2048, lds0, hw, lane);
      const u32x4 g0 = lds8(lds0 + M_GATE + 160 + g * 16), g1 = lds8(lds0 + M_GATE + 224 + g * 16); const u32x2 g2 = lds4(lds0 + M_GATE + 288 + g * 8);
      const unsigned st = lds0 + G_OUT + hw * 2560, xa = st + r * 160;
      *(LAS u32x4*)(xa + g * 16) = gated(y0, g0, c[0], c[1]);
      *(LAS u32x4*)(xa + 64 + g * 16) = gated(y1, g1, c[2], c[3]);
      *(LAS u32x2*)(xa + 128 + g * 8) = gated4(yt, g2, c[4]);
      const rsrc_t orr = mk_rsrc(out + (size_t)pi * 64 * D + 80 * j, 64 * D * 2 - 160 * j);
#pragma unroll
      for (int i = 0; i < 3; ++i) {
        const int ch = i * 64 + lane, row = ch / 10;
        if (ch < 160) bst16(orr, prow(hw, row) * (D * 2) + (ch - row * 10) * 16, 0, lds8(st + ch * 16));
      }
    }
    return;
  }
  {
    __builtin_amdgcn_s_setprio(3);
    unsigned rw[8];
    const int un = p2 * 8 + wv, k8 = un / 3, seg = un - k8 * 3;
    const int oc = seg == 2 ? 2 * lane : (lane < 32 ? (lane >> 3) * DH + 2 * (lane & 7) : ((lane - 32) >> 1) * DH + 16 + 2 * (lane & 1));
    rows8(rw, mk_rsrc(wqkv, un < 240 ? D * 3 * D * 2 : 0), lane < 40 ? (seg * D + 80 * j2 + oc) * 2 : (1 << 28), 3 * D * 2, k8 * 8, k8 * 8 + 4);
    {
      const bool nat = seg == 2 || lane >= 32;
      const int tile = seg == 2 ? 10 + (lane >> 3) : (lane < 32 ? seg * 4 + 2 * (lane >> 4) : 8 + seg), rho = nat ? 2 * (lane & 7) : r;
      const int d0 = (lane < 40 && un < 240) ? WS_WQKV + ((j2 * 15 + tile) * 20 + (k8 >> 2)) * 1024 + ((k8 & 3) * 16 + rho) * 16 : WS_END;
      int e0 = d0, e1 = d0 + (nat ? 16 : 20480); u32x4 c0, c1;
      transp(c0, c1, rw); arrange(e0, e1, nat, lane, c0, c1);
      bst16c(wsr, e0, 0, c0); bst16c(wsr, e1, 0, c1);
    }
    signal_done(sync + 18, 8u, FP(FL_WQ + j2, p2), tag, lane);
    if (wv == 0) { __builtin_amdgcn_s_sleep(PD0); wait_flags<PNAP>(FP(FL_WQ + j2, 0), 30, tag, lane); lds_inc(sync + 15, lane); }
    if (wv >= 3) {
      __builtin_amdgcn_s_sleep(PD1);
      const unsigned th = (unsigned)(tag >> 32), tl = (unsigned)tag >> 24;
      f32x2 t[2];
      for (unsigned spins = 0; spins < (1u << 21); ++spins) {
        u32x4 q[2][4];
#pragma unroll
        for (int i = 0; i < 8; ++i) q[i >> 2][i & 3] = bld16c(wsr, lane * 16, WS_MOD + (i & 3) * (6 * D * 8) + (wv - 3) * 2048 + (i >> 2) * 1024);
        bool ok = true;
#pragma unroll
        for (int i = 0; i < 2; ++i) {
          t[i] = f32x2{0.f, 0.f};
#pragma unroll
          for (int h = 0; h < 4; ++h) {
            ok &= q[i][h][1] == th && q[i][h][3] == th && (q[i][h][0] >> 24) == tl && (q[i][h][2] >> 24) == tl;
            t[i] += f32x2{__uint_as_float(q[i][h][0] << 8), __uint_as_float(q[i][h][2] << 8)};
          }
        }
        if (__all(ok)) break;
        asm volatile("" ::: "memory");
        __builtin_amdgcn_s_sleep(PNAP);
      }
#pragma unroll
      for (int i = 0; i < 2; ++i) {
        const int c0 = (wv - 3) * 256 + i * 128;
        const float one = c0 >= D ? 1.0f : 0.0f;
        *(LAS f32x2*)(lds0 + Q_GS + (c0 + 2 * lane) * 4) = f32x2{rbf(rbf(t[i][0]) + one), rbf(rbf(t[i][1]) + one)};
      }
      asm volatile("s_waitcnt lgkmcnt(0)" ::: "memory");
      lds_inc(sync + 13, lane);
    }
    wait_lds<1>(sync + 15, 1u);
    __builtin_amdgcn_s_setprio(1);
    const int qr = (wv + 5) & 7;
    if (qr == 7) qkv_mma<true, 1>(ws, lds0, sync + 24, qr, p2, j2);
    else if (qr >= 5) qkv_mma<true, 2>(ws, lds0, sync + 24, qr, p2, j2);
    else qkv_mma<false, 2>(ws, lds0, sync + 24, qr, p2, j2);
    if (qr >= 5) { asm volatile("s_waitcnt lgkmcnt(0)" ::: "memory"); lds_inc(sync + 21, lane); }
    signal_done(sync + 16, 12u, FP(FL_QKV + j2, p2), tag, lane);
    if (wv == 2) {
      unsigned mv[5];
      get_mod<5, 8>(mv, mod, tag, [&](int i) {
        if (i < 3) { const int e = min(i * 64 + lane, 159); return (e < 80 ? 2 * D + e : 5 * D + e - 80) + 80 * j; }
        const int k16 = min((BID & 31) * 2 + i - 3, 39);
        int k0, k1; bool ok; krows<80, 2>(k16 >> 1, 2 * (k16 & 1) + ((lane >> 3) & 1), k0, k1, ok);
        return ((lane & 16) ? 3 : 4) * D + ((lane & 4) ? k1 : k0) + (lane & 3);
      });
#pragma unroll
      for (int i = 0; i < 3; ++i) *(LAS u16*)(lds0 + M_GATE + min(i * 64 + lane, 159) * 2) = (u16)mv[i];
      *(LAS u16*)(lds0 + Q_PM + lane * 2) = (u16)mv[3]; *(LAS u16*)(lds0 + Q_PM + 128 + lane * 2) = (u16)mv[4];
      asm volatile("s_waitcnt lgkmcnt(0)" ::: "memory");
      lds_inc(sync + 19, lane);
    }
  }
  __builtin_amdgcn_s_setprio(0);
  const int qg = wv & 3, kh = wv >> 2;
  const int q0 = qb * 256 + qg * 64;
  const int pk = (r32 & 7) | ((r32 & 8) << 1) | ((r32 & 16) >> 1);
  const int pk8o = (pk & 15) * 16 + (pk >> 4) * 8;
  wait_lds<1>(sync + 14, 1u);
  const frag qaA = bld16c(wsr, ((head * 2 + hi) * S + q0 + r32) * 16, WS_QA), qaB = bld16c(wsr, ((head * 2 + hi) * S + q0 + r32) * 16, WS_QA + 512);
  u32x2 qbA = bld8c(wsr, (head * S + q0) * 8 + r * 16 + (r32 >> 4) * 8, WS_QB), qbB = bld8c(wsr, (head * S + q0) * 8 + r * 16 + (r32 >> 4) * 8, WS_QB + 256);
  const unsigned s0 = lds0 + 2 * AT_SLOT, kz = s0 + hi * (AT_C * 16) + pk * 16, bz = s0 + AT_KB + pk8o;
  const unsigned ka0 = hi * (AT_C * 16) + pk * 16 + kh * 4096, kb0 = AT_KB + pk8o + kh * 2048;
  const unsigned v0 = AT_V + (hi * NV + min(r32, NV - 1)) * 16 + kh * (32 * NV * 16);
  if (hi) { qbA = u32x2{0u, 0u}; qbB = u32x2{0u, 0u}; }
  PIN(qbA); PIN(qbB);
  wait_lds<1>(sync + 20, 4u);
  f32x16 oA = {}, oB = {}, nmA, nmB, sA, sB;
  frag ka, va, vb, plo, phi = {0u, 0u, 0u, 0u}; u32x2 kb;
  {
    ka = lds8(kz); kb = lds4(bz);
    sA = mfma32(ka, qaA, f32x16{}); sB = mfma32(ka, qaB, f32x16{});
    sA = mfma32k8(kb, qbA, sA); sB = mfma32k8(kb, qbB, sB);
    ka = lds8(s0 + ka0); kb = lds4(s0 + kb0);
    const float mA = max16(sA), mB = max16(sB);
#pragma unroll
    for (int e = 0; e < 16; ++e) { nmA[e] = -mA; nmB[e] = -mB; }
    PIN(nmA); PIN(nmB);
    sA = mfma32(ka, qaA, nmA); sA = mfma32k8(kb, qbA, sA);
    wait_lds<1>(sync, 4u);
    vb = lds8(s0 + v0); va = lds8(s0 + v0);
  }
#define PRIO(i) do { if ((((PMASK >> (i)) ^ (PMASK >> (((i) + 15) & 15))) & 1) != 0) { \
    asm volatile("s_cmp_eq_u32 %0, 0\n\ts_cbranch_scc1 1\n\ts_setprio %1" :: "s"(kh), "i"(((PMASK >> (i)) & 1) ? 0 : 1) : "scc"); SBAR(); } } while (0)
#define EX4(C, i, P, w) do { C[i] = __builtin_amdgcn_exp2f(C[i]); C[i + 1] = __builtin_amdgcn_exp2f(C[i + 1]); C[i + 2] = __builtin_amdgcn_exp2f(C[i + 2]); \
    C[i + 3] = __builtin_amdgcn_exp2f(C[i + 3]); P[w] = pk2(C[i], C[i + 1]); P[w + 1] = pk2(C[i + 2], C[i + 3]); PIN(P); SBAR(); } while (0)
  for (int ch = 0; ch < 4; ++ch) {
    const unsigned cur = lds0 + ((ch + 2) % 3) * AT_SLOT, nxt = lds0 + (ch % 3) * AT_SLOT;
    const unsigned kc = cur + ka0, kn = nxt + ka0, bc = cur + kb0, bn = nxt + kb0, vc = cur + v0, vn = nxt + v0;
#pragma unroll
    for (int t = 0; t < 8; ++t) {
      if (t == 7 && ch < 3) wait_lds<4>(sync + ch + 1, 4u);
      if (t == 1) { if (ch >= 1) lds_inc(sync + 3 + ch, lane); }
      PRIO(2 * t);
      sB = mfma32(ka, qaB, nmB); SBAR(); ka = lds8(t < 7 ? kc + (t + 1) * 512 : kn); SBAR(); EX4(sA, 0, plo, 0);
      oB = mfma32(vb, phi, oB); SBAR(); vb = lds8(vc + (4 * t + 2) * (NV * 16)); SBAR(); EX4(sA, 4, plo, 2);
      sB = mfma32k8(kb, qbB, sB); SBAR(); kb = lds4(t < 7 ? bc + (t + 1) * 256 : bn); SBAR(); EX4(sA, 8, phi, 0);
      oA = mfma32(va, plo, oA); SBAR(); EX4(sA, 12, phi, 2);
      PRIO(2 * t + 1);
      sA = mfma32(ka, qaA, nmA); SBAR(); EX4(sB, 0, plo, 0);
      oA = mfma32(vb, phi, oA); SBAR(); EX4(sB, 4, plo, 2);
      sA = mfma32k8(kb, qbA, sA); SBAR(); EX4(sB, 8, phi, 0);
      oB = mfma32(va, plo, oB); SBAR(); va = lds8(t < 7 ? vc + (4 * t + 4) * (NV * 16) : vn); SBAR(); EX4(sB, 12, phi, 2);
    }
  }
  oB = mfma32(vb, phi, oB);
#undef EX4
  f32x16 o;
  {
    PIN(oA); PIN(oB);
    const unsigned rw = lds0 + AT_RED + wv * 3072 + lane * 16, rd = lds0 + AT_RED + (wv ^ 4) * 3072 + lane * 16;
#pragma unroll
    for (int i = 0; i < 3; ++i) {
      f32x4 w;
#pragma unroll
      for (int e = 0; e < 4; ++e) { w[e] = kh ? oA[4 * i + e] : oB[4 * i + e]; o[4 * i + e] = kh ? oB[4 * i + e] : oA[4 * i + e]; }
      *(LAS f32x4*)(rw + i * 1024) = w;
    }
    BAR();
#pragma unroll
    for (int i = 0; i < 3; ++i) {
      const f32x4 w = *(const LAS f32x4*)(rd + i * 1024);
#pragma unroll
      for (int e = 0; e < 4; ++e) o[4 * i + e] += w[e];
    }
  }
  float l = __shfl(o[8], r32 + 32);
  float chk = 0.f;
#pragma unroll
  for (int e = 0; e < 12; ++e) chk += __builtin_fabsf(o[e]);
  if (__builtin_expect(__any(!(chk < 1.0e30f)), 0)) {
    const frag qa = kh ? qaB : qaA; const u32x2 qb4 = kh ? qbB : qbA;
    o = f32x16{};
    float m = -INFINITY;
    for (int t = 0; t < 64; ++t) {
      const frag k1 = bld16c(wsr, (hi * S + t * 32 + pk) * 16, WS_KA + head * (S * 32));
      const u32x2 k2 = bld8c(wsr, t * 256 + pk8o, WS_KB + head * (S * 8));
      const int vp = ((t * 4 + hi) * NV + min(r32, NV - 1)) * 16;
      const frag v1 = bld16c(wsr, vp, WS_VT + head * (256 * NV * 16)), v2 = bld16c(wsr, vp + 2 * NV * 16, WS_VT + head * (256 * NV * 16));
      f32x16 s = mfma32(k1, qa, f32x16{}); s = mfma32k8(k2, qb4, s);
      const float mn = __builtin_fmaxf(m, max16(s));
      const float al = __builtin_amdgcn_exp2f(m - mn);
      m = mn;
#pragma unroll
      for (int e = 0; e < 16; ++e) { s[e] = __builtin_amdgcn_exp2f(s[e] - mn); o[e] *= al; }
      o = mfma32(v1, pack8(s, 0), o); o = mfma32(v2, pack8(s, 8), o);
    }
    l = __shfl(o[8], r32 + 32);
  }
  const float rl = __builtin_amdgcn_rcpf(l);
  const int pos = 32 * kh + r32;
  const int vo = (qb * 4 + qg) * (80 * 1024) + (pos >> 4) * 1024 + (pos & 15) * 16;
  bst16c(wsr, vo + hi * 256, WS_O + (head >> 1) * 4096 + (head & 1) * 512,
         u32x4{pk2(o[0] * rl, o[1] * rl), pk2(o[2] * rl, o[3] * rl), pk2(o[4] * rl, o[5] * rl), pk2(o[6] * rl, o[7] * rl)});
  if (!hi) bst8c(wsr, (qb * 4 + qg) * 81920 + kh * 256 + (pos & 15) * 16 + ((pos >> 4) & 1) * 8, WS_O + 65536 + head * 512,
                 u32x2{pk2(o[8] * rl, o[9] * rl), pk2(o[10] * rl, o[11] * rl)});
  __builtin_amdgcn_s_setprio(3);
  signal_done(sync + 8, 8u, FP(FL_O + qb, head), tag, lane);
  GAS unsigned long long* gran = (GAS unsigned long long*)(ws + WS_STAT) + pi * 512;
  if (wv == 7) {
    wait_lds<2>(sync + 8, 8u); __builtin_amdgcn_s_sleep(DO);
    wait_flags<FNAP>(FP(FL_O + (pi >> 2), 0), 32, tag, lane);
    RAWBAR();
    wait_lds<2>(sync + 9, 4u);
    __builtin_amdgcn_s_sleep(DX);
    wait_flags<FNAP>(FP(FL_X1 + pi, 0), 8, tag, lane);
    RAWBAR();
    {
      u32x2 xs[8];
      RAWBAR();
#pragma unroll
      for (int i = 0; i < 8; ++i) xs[i] = bld8c(wsr, lane * 8, WS_XST + pi * 4096 + i * 512);
      RAWBAR();
      float s1 = 0.f, s2 = 0.f;
#pragma unroll
      for (int i = 0; i < 8; ++i) { PIN(xs[i]); s1 += __uint_as_float(xs[i][0]); s2 += __uint_as_float(xs[i][1]); }
      const float mu = s1 * (1.0f / D);
      const float rs = __builtin_amdgcn_rsqf(__builtin_fmaxf(s2 * (1.0f / D) - mu * mu, 0.f) + 1e-5f);
      lnp[lane] = rs; lnp[64 + lane] = -mu * rs;
      BAR();
    }
    wait_lds<1>(sync + 10, 7u);
    float t1 = 0.f, t2 = 0.f;
    {
      f32x4 p[14];
#pragma unroll
      for (int i = 0; i < 14; ++i) p[i] = *(const LAS f32x4*)(partb + lane * 32 + (i >> 1) * 2048 + (i & 1) * 16);
      SBAR();
#pragma unroll
      for (int i = 1; i < 14; ++i) p[0] += p[i];
      t1 = p[0][0] + p[0][2]; t2 = p[0][1] + p[0][3];
    }
    unsigned b1w = __float_as_uint(t1), b2w = __float_as_uint(t2);
    if (b2w == ~0u) b2w = 0x7fc00000u;
    __hip_atomic_store(gran + j * 64 + lane, ((unsigned long long)b2w << 32) | b1w, __ATOMIC_RELAXED, __HIP_MEMORY_SCOPE_AGENT);
    __builtin_amdgcn_s_sleep(DE);
    for (unsigned spins = 0;; ++spins) {
      bool ok = true; t1 = 0.f; t2 = 0.f;
#pragma unroll
      for (int w = 0; w < 8; ++w) {
        unsigned long long v = __hip_atomic_load(gran + w * 64 + lane, __ATOMIC_RELAXED, __HIP_MEMORY_SCOPE_AGENT);
        ok &= (unsigned)(v >> 32) != ~0u;
        t1 += __uint_as_float((unsigned)v); t2 += __uint_as_float((unsigned)(v >> 32));
      }
      if (__all(ok) || spins > (1u << 20)) break;
      __builtin_amdgcn_s_sleep(1);
    }
    const float mu = t1 * (1.0f / FH);
    const float rs = __builtin_amdgcn_rsqf(__builtin_fmaxf(t2 * (1.0f / FH) - mu * mu, 0.f) + 1e-5f);
    znp[lane] = rs; znp[64 + lane] = -mu * rs;
    BAR();
    RAWBAR();
    __builtin_amdgcn_s_sleep(DZ);
    wait_flags<FNAP>(FP(FL_Z + j, 0), 32, tag, lane);
  } else {
  __builtin_amdgcn_s_setprio(1);
  const rsrc_t wr = mk_rsrc(ws + WS_WIN + (j * 14 + wv) * 20 * 1024, 8 * 20 * 1024);
  frag bw[40], a[3][4];
  auto ldb = [&](int i) { if (i < 40) bw[i] = bld16c(wr, lane * 16, (i < 20 ? i : 120 + i) * 1024); };
  auto upto = [](int ks) { return ks <= 12 ? 20 : (ks <= 20 ? 8 + ks : 28 + 2 * (ks - 20)); };
  constexpr int WB = 20;
#pragma unroll
  for (int i = 0; i < 8; ++i) ldb(i);
  RAWBAR();
  __builtin_amdgcn_s_sleep(24);
#pragma unroll
  for (int i = 8; i < WB; ++i) ldb(i);
  RAWBAR();
  f32x4 z2[4] = {}, z1[4] = {};
  unsigned ab = lds0 + M_IMG + lane * 16, ab2 = lds0 + M_IMG + 65536 + lane * 8, ab3 = ab2 + 2048;
  PIN(ab); PIN(ab2); PIN(ab3);
  auto step = [&](int ks) {
    const int kl = ks + 2;
    if (kl == 0 || kl == 4 || kl == 12) RAWBAR();
#pragma unroll
    for (int mt = 0; mt < 4; ++mt) {
      if (kl < 40) {
        if (kl % 20 < 16) a[kl % 3][mt] = lds8(ab + (kl % 20) * 4096 + mt * 1024);
        else {
          const u32x2 lo = lds4(ab2 + (kl % 4) * 4096 + mt * 512), hi = lds4(ab3 + (kl % 4) * 4096 + mt * 512);
          a[kl % 3][mt] = frag{lo[0], lo[1], hi[0], hi[1]};
        }
        SBAR();
      }
      if (ks >= 0) {
        if (ks < 20) z2[mt] = mfma16(a[ks % 3][mt], bw[ks], z2[mt]);
        else z1[mt] = mfma16(bw[ks], a[ks % 3][mt], z1[mt]);
        SBAR();
      }
    }
    if (ks < 0) return;
#pragma unroll
    for (int i = upto(ks); i < upto(ks + 1); ++i) ldb(i);
    SBAR();
  };
#pragma unroll
  for (int ks = -2; ks < 18; ++ks) step(ks);
  f32x4 lrs[4], lnb[4];
  {
    const float u = uvp[wv * 16 + r], v = uvp[224 + wv * 16 + r];
#pragma unroll
    for (int mt = 0; mt < 4; ++mt) { lrs[mt] = *(const LAS f32x4*)&lnp[mt * 16 + 4 * g]; lnb[mt] = *(const LAS f32x4*)&lnp[64 + mt * 16 + 4 * g]; }
    step(18);
#pragma unroll
    for (int mt = 0; mt < 4; ++mt) lnb[mt] = __builtin_elementwise_fma(lnb[mt], splat(u), splat(v));
    step(19);
  }
  {
    {
      __builtin_amdgcn_s_setprio(2);
#pragma unroll
      for (int mt = 0; mt < 4; ++mt) {
        z2[mt] = gelu4(__builtin_elementwise_fma(z2[mt], lrs[mt], lnb[mt]));
        float sa, sb;
        quad_part(sa, sb, z2[mt], z2[mt] * z2[mt]);
        *(LAS f32x2*)(partb + wv * 2048 + mt * 512 + lane * 8) = f32x2{sa, sb};
      }
      asm volatile("s_waitcnt lgkmcnt(0)" ::: "memory");
      lds_inc(sync + 10, lane);
      __builtin_amdgcn_s_setprio(1);
    }
#pragma unroll
    for (int ks = 20; ks <= ZSPLIT; ++ks) step(ks);
    {
      RAWBAR();
#pragma unroll
      for (int mt = 0; mt < 4; ++mt)
        z2[mt] = __builtin_elementwise_fma(z2[mt], *(const LAS f32x4*)&znp[mt * 16 + 4 * g], *(const LAS f32x4*)&znp[64 + mt * 16 + 4 * g]);
      const int zo = ((j * 7 + wv) * 64 + pi * 2) * 1024 + lane * 16;
      bst16c(wsr, zo, WS_ZT, pk8(z2[0], z2[1])); bst16c(wsr, zo, WS_ZT + 1024, pk8(z2[2], z2[3]));
      SBAR();
    }
#pragma unroll
    for (int ks = ZSPLIT + 1; ks <= ZSPLIT + ZDRAIN; ++ks) step(ks);
    signal_done(sync + 11, 7u, FP(FL_Z + j, pi), tag, lane);
#pragma unroll
    for (int ks = ZSPLIT + ZDRAIN + 1; ks < 40; ++ks) step(ks);
  }
  {
    const f32x4 u4 = *(const LAS f32x4*)&uvp[112 + wv * 16 + 4 * g], v4 = *(const LAS f32x4*)&uvp[336 + wv * 16 + 4 * g];
#pragma unroll
    for (int mt = 0; mt < 4; ++mt)
      z1[mt] = gelu4(__builtin_elementwise_fma(z1[mt], splat(lnp[mt * 16 + r]), __builtin_elementwise_fma(splat(lnp[64 + mt * 16 + r]), u4, v4)));
    *(LAS u32x4*)(lds0 + G_RED + wv * 2048 + lane * 16) = pk8(z1[0], z1[1]);
    *(LAS u32x4*)(lds0 + G_RED + wv * 2048 + 1024 + lane * 16) = pk8(z1[2], z1[3]);
  }
  BAR();
  }
  {
    __builtin_amdgcn_s_setprio(0);
    const int p = wv >> 1, kp = wv & 1;
    u32x4 z1v[2]; unsigned bsv[2];
#pragma unroll
    for (int i = 0; i < 2; ++i) {
      bsv[i] = b_sp[pi * 64 + prow(2 * kp + i, r)];
      z1v[i] = lds8(lds0 + G_RED + min(2 * p + i, 6) * 2048 + kp * 1024 + lane * 16);
    }
    const rsrc_t br = mk_rsrc(ws + WS_ZT + (j * 7 + 2 * p) * 65536, 2 * 65536);
    const int voff = lane * 16 + kp * 1024;
    const unsigned ab = afrag_base(lds0, lane, kp);
    f32x4 acc[2][4] = {};
    BAR();
    if (p < 2) sp_loop<2, true>(acc, br, voff, ab, rot);
    else if (p == 2) sp_loop<2, false>(acc, br, voff, ab, rot);
    else sp_loop<1, false>(acc, br, voff, ab, rot);
    __builtin_amdgcn_s_setprio(3);
    const unsigned rw = lds0 + G_RED + (wv < 7 ? wv * 4096 : 26624) + lane * 16, rd = lds0 + G_RED + ((wv ^ 1) < 7 ? (wv ^ 1) * 4096 : 26624) + lane * 16;
    f32x4 c[2][2];
#pragma unroll
    for (int t = 0; t < 2; ++t)
#pragma unroll
      for (int i = 0; i < 2; ++i) {
        f32x4 lo = acc[t][i], hi = acc[t][2 + i];
        PIN(lo); PIN(hi);
        if (t == 0 || p < 3) *(LAS f32x4*)(rw + (t * 2 + i) * 1024) = kp ? lo : hi;
        c[t][i] = kp ? hi : lo;
      }
    BAR();
#pragma unroll
    for (int t = 0; t < 2; ++t)
#pragma unroll
      for (int i = 0; i < 2; ++i) c[t][i] += *(const LAS f32x4*)(rd + (t * 2 + i) * 1024);
    f32x4 y[2][2];
#pragma unroll
    for (int i = 0; i < 2; ++i) {
      const float bs = bf_lo(bsv[i]);
      y[0][i] = up4(z1v[0][2 * i], z1v[0][2 * i + 1]) * (c[0][i] + bs); y[1][i] = up4(z1v[1][2 * i], z1v[1][2 * i + 1]) * (c[1][i] + bs);
    }
    if (p < 3) {
      bst16c(wsr, (pi * 28 + 3 * j + p) * 4096 + kp * 2048 + lane * 16, WS_U, pkI(y[0][0], y[1][0]));
      bst16c(wsr, (pi * 28 + 3 * j + p) * 4096 + kp * 2048 + lane * 16, WS_U + 1024, pkI(y[0][1], y[1][1]));
    } else {
      bst16c(wsr, (pi * 28 + 24) * 4096 + (2 * j + kp) * 1024 + lane * 16, WS_U, pk8(y[0][0], y[0][1]));
    }
    signal_done(sync + 12, 8u, FP(FL_U + pi, j), tag, lane);
    if (wv == 0) { wait_lds<2>(sync + 12, 8u); __builtin_amdgcn_s_sleep(DU); wait_flags<FNAP>(FP(FL_U + pi, 0), 8, tag, lane); }
  }
}

extern "C" void kernel_launch(void* const* d_in, const int* in_sizes, int n_in, void* d_out, int out_size, void* d_ws,
                              size_t ws_size, hipStream_t stream) {
  (void)hipFuncSetAttribute((const void*)k_all, hipFuncAttributeMaxDynamicSharedMemorySize, AT_LDS);
  k_all<<<256, 768, AT_LDS, stream>>>((const u16*)d_in[9], (const u16*)d_in[8], (const u16*)d_in[1], (const u16*)d_in[0], (char*)d_ws,
                                      (const u16*)d_in[4], (const u16*)d_in[2], (const u16*)d_in[7], (const u16*)d_in[5],
                                      (const u16*)d_in[6], (u16*)d_out);
}
```
